```python
import torch

def _softmax(x, axis=-1):
    return torch.softmax(x, dim=axis)
def _gelu(x):
    return torch.nn.functional.gelu(x, approximate="tanh")
def _silu(x):
    return torch.nn.functional.silu(x)
def _sigmoid(x):
    return torch.sigmoid(x)
def _layernorm(x, axis=-1, eps=1e-5):
    m = x.mean(dim=axis, keepdim=True)
    v = x.var(dim=axis, keepdim=True, unbiased=False)
    return (x - m) / torch.sqrt(v + eps)
def _scatter_add(tgt, idx, src, axis=0):
    return tgt.clone().index_add_(axis, idx, src)
def _segment_sum(src, seg, n):
    out = torch.zeros((n,) + src.shape[1:], dtype=src.dtype)
    return out.index_add_(0, seg, src)
def _pad(x, pw):
    flat = tuple(v for pair in reversed(pw) for v in pair)
    return torch.nn.functional.pad(x, flat)

kernel_name = 'gated_mix__spectral_gfnet__dense_activation_gelu__prompt'

def setup_inputs():

    _w = (lambda a: (lambda t: t.to(torch.bfloat16) if t.is_floating_point() else t)(torch.as_tensor(a)))
    def _A_setup():
        B, S, D = 1, 2048, 1024
        Df = D // 2 + 1
        return {
            "x": _w((lambda *s: torch.randn(*s, dtype=torch.bfloat16) * 0.05)(B, S, D)),
            "w_r": _w((lambda *s: torch.randn(*s, dtype=torch.bfloat16) * 3.2)(S, Df) * 0.02),
            "w_i": _w((lambda *s: torch.randn(*s, dtype=torch.bfloat16) * 3.2)(S, Df) * 0.02),
        }
    _A_in = _A_setup()

    def _B_setup():
        B, S, D, F = 1, 2048, 1024, 3584
        return {
            "x": _w((lambda *s: torch.randn(*s, dtype=torch.bfloat16) * 0.05)(B, S, D)),
            "w1": _w((lambda *s: torch.randn(*s, dtype=torch.bfloat16) * 3.2)(D, F) * (D ** -0.5)),
            "b1": _w((lambda *s: torch.randn(*s, dtype=torch.bfloat16) * 3.2)(F) * 0.01),
            "w2": _w((lambda *s: torch.randn(*s, dtype=torch.bfloat16) * 0.2)(F, D) * (F ** -0.5)),
            "b2": _w((lambda *s: torch.randn(*s, dtype=torch.bfloat16) * 0.4)(D) * 0.01),
        }
    _B_in = _B_setup()

    return {
        "A_x": _A_in["x"],
        "A_w_r": _A_in["w_r"],
        "A_w_i": _A_in["w_i"],
        "B_x": _B_in["x"],
        "B_w1": _B_in["w1"],
        "B_b1": _B_in["b1"],
        "B_w2": _B_in["w2"],
        "B_b2": _B_in["b2"],
        "gate_w": _w((lambda *s: torch.randn(*s, dtype=torch.bfloat16) * 0.05)(1024) * 16.0),
    }

def reference(A_x, A_w_r, A_w_i, B_x, B_w1, B_b1, B_w2, B_b2, gate_w):
    def _A_ref(x, w_r, w_i):
        B, S, D = x.shape
        X = (lambda x, axes=(-2,-1): torch.fft.rfft2(x.float(), dim=axes))(x, axes=(-2, -1))
        W = w_r + 1j * w_i
        X = X * W
        y = (lambda x, s, axes=(-2,-1): torch.fft.irfft2(x, s=s, dim=axes).to(torch.bfloat16))(X, (S, D), axes=(-2, -1))
        return y

    def _B_ref(x, w1, b1, w2, b2):
        h = torch.einsum("bsd,df->bsf", x, w1) + b1
        h = _gelu(h)
        return torch.einsum("bsf,fd->bsd", h, w2) + b2

    x = A_x
    g = _sigmoid((x * gate_w).sum(-1, keepdims=True))
    a = _A_ref(x, A_w_r, A_w_i)
    b = _B_ref(x, B_w1, B_b1, B_w2, B_b2)
    return g * a + (1 - g) * b
```

```cpp
#include <hip/hip_runtime.h>
#include <hip/hip_bf16.h>
typedef unsigned short u16;
__device__ __forceinline__ float bf2f(u16 v){ return __uint_as_float(((unsigned)v)<<16); }
__device__ __forceinline__ u16 f2bf(float f){ unsigned u=__float_as_uint(f); u += 0x7fffu + ((u>>16)&1u); return (u16)(u>>16); }
__device__ __forceinline__ float rbf(float f){ return bf2f(f2bf(f)); }
constexpr int S=2048, D=1024, F=3584, DF=513;

__global__ void k_gate(const u16* x, const u16* gw, float* g){
  int s = blockIdx.x; float acc=0;
  for(int d=threadIdx.x; d<D; d+=64) acc += rbf(bf2f(x[s*D+d])*bf2f(gw[d]));
  for(int o=32;o>0;o>>=1) acc += __shfl_xor(acc,o);
  if(threadIdx.x==0){ float z=rbf(acc); g[s]=rbf(1.f/(1.f+__expf(-z))); }
}
template<int MODE>
__global__ void k_gemm(const u16* A, const u16* B, const u16* bias, u16* C, int M,int N,int K){
  __shared__ float As[16][17], Bs[16][17];
  int tx=threadIdx.x&15, ty=threadIdx.x>>4;
  int row=blockIdx.y*16+ty, col=blockIdx.x*16+tx; float acc=0;
  for(int k0=0;k0<K;k0+=16){
    As[ty][tx]=bf2f(A[row*K+k0+tx]); Bs[ty][tx]=bf2f(B[(k0+ty)*N+col]); __syncthreads();
    for(int k=0;k<16;k++) acc+=As[ty][k]*Bs[k][tx];
    __syncthreads();
  }
  float v=rbf(rbf(acc)+bf2f(bias[col]));
  if(MODE==1){ float u=0.7978845608f*(v+0.044715f*v*v*v); v=0.5f*v*(1.f+tanhf(u)); }
  C[row*N+col]=f2bf(v);
}
__global__ void k_rowdft(const u16* x, float2* X1){
  __shared__ float xs[D]; __shared__ float2 tw[D];
  int s=blockIdx.x;
  for(int d=threadIdx.x; d<D; d+=blockDim.x){ xs[d]=bf2f(x[s*D+d]); float sn,cs; sincospif(2.f*d/D,&sn,&cs); tw[d]=make_float2(cs,-sn);}
  __syncthreads();
  for(int k=threadIdx.x;k<DF;k+=blockDim.x){ float re=0,im=0; for(int d=0;d<D;d++){ float2 w=tw[(d*k)&(D-1)]; re+=xs[d]*w.x; im+=xs[d]*w.y;} X1[s*DF+k]=make_float2(re,im);}
}
__global__ void k_coldft(const float2* X, float2* Y, const u16* wr, const u16* wi, float sgn){
  __shared__ float2 xs[S]; __shared__ float2 tw[S];
  int k=blockIdx.x;
  for(int s=threadIdx.x;s<S;s+=blockDim.x){ xs[s]=X[s*DF+k]; float sn,cs; sincospif(2.f*s/S,&sn,&cs); tw[s]=make_float2(cs,sgn*sn);}
  __syncthreads();
  for(int j=threadIdx.x;j<S;j+=blockDim.x){ float re=0,im=0; for(int s=0;s<S;s++){ float2 w=tw[(s*j)&(S-1)]; float2 v=xs[s]; re+=v.x*w.x-v.y*w.y; im+=v.x*w.y+v.y*w.x;}
    if(wr){ float a=bf2f(wr[j*DF+k]), b=bf2f(wi[j*DF+k]); float r2=re*a-im*b, i2=re*b+im*a; re=r2; im=i2;}
    Y[j*DF+k]=make_float2(re,im);}
}
__global__ void k_rowidft_mix(const float2* Z, const float* g, const u16* b, u16* out){
  __shared__ float2 zs[DF]; __shared__ float2 tw[D];
  int s=blockIdx.x;
  for(int k=threadIdx.x;k<DF;k+=blockDim.x) zs[k]=Z[s*DF+k];
  for(int d=threadIdx.x; d<D; d+=blockDim.x){ float sn,cs; sincospif(2.f*d/D,&sn,&cs); tw[d]=make_float2(cs,sn);}
  __syncthreads();
  for(int d=threadIdx.x; d<D; d+=blockDim.x){
    float acc=zs[0].x + ((d&1)?-zs[512].x:zs[512].x);
    for(int k=1;k<512;k++){ float2 w=tw[(d*k)&(D-1)]; acc+=2.f*(zs[k].x*w.x-zs[k].y*w.y);}
    float a=rbf(acc*(1.f/(S*(float)D)));
    float gg=g[s]; float o=rbf(rbf(gg*a)+rbf(rbf(1.f-gg)*bf2f(b[s*D+d])));
    out[s*D+d]=f2bf(o);
  }
}
extern "C" void kernel_launch(void* const* d_in, const int* in_sizes, int n_in, void* d_out, int out_size, void* d_ws, size_t ws_size, hipStream_t stream){
  const u16* x=(const u16*)d_in[0]; const u16* wr=(const u16*)d_in[1]; const u16* wi=(const u16*)d_in[2];
  const u16* w1=(const u16*)d_in[4]; const u16* b1=(const u16*)d_in[5]; const u16* w2=(const u16*)d_in[6]; const u16* b2=(const u16*)d_in[7]; const u16* gw=(const u16*)d_in[8];
  char* ws=(char*)d_ws;
  float* g=(float*)ws; u16* h=(u16*)(ws+(1<<20)); u16* b=(u16*)(ws+(20<<20)); float2* X1=(float2*)(ws+(32<<20)); float2* X2=(float2*)(ws+(48<<20)); float2* X3=(float2*)(ws+(64<<20));
  k_gate<<<S,64,0,stream>>>(x,gw,g);
  k_gemm<1><<<dim3(F/16,S/16),256,0,stream>>>(x,w1,b1,h,S,F,D);
  k_gemm<0><<<dim3(D/16,S/16),256,0,stream>>>(h,w2,b2,b,S,D,F);
  k_rowdft<<<S,256,0,stream>>>(x,X1);
  k_coldft<<<DF,256,0,stream>>>(X1,X2,wr,wi,-1.f);
  k_coldft<<<DF,256,0,stream>>>(X2,X3,nullptr,nullptr,1.f);
  k_rowidft_mix<<<S,256,0,stream>>>(X3,g,b,(u16*)d_out);
}
```

```cpp
#include <hip/hip_runtime.h>
#include <stdint.h>
typedef unsigned short u16;
#define LAS __attribute__((address_space(3)))
#define DI __device__ __forceinline__
typedef short s16x4 __attribute__((ext_vector_type(4)));
typedef short s16x8 __attribute__((ext_vector_type(8)));
typedef __bf16 bf16x8 __attribute__((ext_vector_type(8)));
typedef __bf16 bf16x2 __attribute__((ext_vector_type(2)));
typedef float f32x2 __attribute__((ext_vector_type(2)));
typedef float f32x4 __attribute__((ext_vector_type(4)));
typedef float f32x16 __attribute__((ext_vector_type(16)));
typedef unsigned u32x2 __attribute__((ext_vector_type(2)));
typedef unsigned u32x4 __attribute__((ext_vector_type(4)));
typedef __amdgpu_buffer_rsrc_t rsrc_t;

constexpr int S_ = 2048, D_ = 1024, F_ = 3584, DF_ = 513;

namespace ct {
constexpr double PI = 3.14159265358979323846;
constexpr double ccos(double x) { double x2 = x * x, t = 1, s = 1; for (int i = 1; i < 16; i++) { t *= -x2 / ((2 * i - 1) * (2 * i)); s += t; } return s; }
constexpr double cos256(int m) { m &= 255; if (m > 128) m = 256 - m; bool neg = false; if (m > 64) { m = 128 - m; neg = true; } double v = (m == 64) ? 0.0 : ccos(2 * PI * m / 256.0); return neg ? -v : v; }
constexpr u16 tobf(double d) { float f = (float)d; unsigned u = __builtin_bit_cast(unsigned, f); u += 0x7fffu + ((u >> 16) & 1u); return (u16)(u >> 16); }
struct Tab { u16 c[256]; };
constexpr Tab mktab() { Tab t{}; for (int i = 0; i < 256; i++) t.c[i] = tobf(cos256(i)); return t; }
constexpr Tab COS = mktab();
constexpr u16 E(int dp, int a) { if (dp == 0) return COS.c[0]; if (dp == 1) return COS.c[(128 * a) & 255];
  int dl = dp >> 1; int m = (dl * a) & 255; return (dp & 1) ? COS.c[(m + 64) & 255] : COS.c[m]; }
struct Chunk { u16 v[8192]; };
template <bool TR> constexpr Chunk mk(int ch) { Chunk c{}; for (int i = 0; i < 8192; i++) { int j = i & 7, lane = (i >> 3) & 63, kb = (i >> 9) & 7, blk = ch * 2 + (i >> 12);
  int col = blk * 16 + (lane & 15); int k = 32 * kb + 16 * (j >> 2) + 4 * (lane >> 4) + (j & 3); c.v[i] = TR ? E(k, col) : E(col, k); } return c; }
template <bool TR, int I> constexpr Chunk CH = mk<TR>(I);
struct Big { Chunk c[8]; };
}
__device__ __attribute__((aligned(1024))) const ct::Big ETAB1 = {{ct::CH<false,0>, ct::CH<false,1>, ct::CH<false,2>, ct::CH<false,3>, ct::CH<false,4>, ct::CH<false,5>, ct::CH<false,6>, ct::CH<false,7>}};
__device__ __attribute__((aligned(1024))) const ct::Big ETAB2 = {{ct::CH<true,0>, ct::CH<true,1>, ct::CH<true,2>, ct::CH<true,3>, ct::CH<true,4>, ct::CH<true,5>, ct::CH<true,6>, ct::CH<true,7>}};

DI float bf2f(u16 v) { return __uint_as_float(((unsigned)v) << 16); }
DI unsigned pk2(float lo, float hi) { f32x2 v = {lo, hi}; bf16x2 b = __builtin_convertvector(v, bf16x2); return __builtin_bit_cast(unsigned, b); }
DI float rbf(float f) { return __uint_as_float(pk2(f, 0.f) << 16); }
DI rsrc_t mkrs(const void* p, unsigned bytes) { return __builtin_amdgcn_make_buffer_rsrc((void*)p, 0, bytes, 0x00020000); }
DI void dma16(rsrc_t rs, LAS char* dst, unsigned voff, unsigned soff) {
  asm volatile("s_mov_b32 m0, %0\n\ts_nop 0\n\tbuffer_load_dwordx4 %1, %2, %3 offen lds" :: "s"((unsigned)(size_t)dst), "v"(voff), "s"(rs), "s"(soff) : "memory");
}
DI s16x4 trd(LAS char* p) { return __builtin_amdgcn_ds_read_tr16_b64_v4i16((LAS s16x4*)p); }
DI bf16x8 cat8(s16x4 a, s16x4 b) { return __builtin_bit_cast(bf16x8, __builtin_shufflevector(a, b, 0, 1, 2, 3, 4, 5, 6, 7)); }
DI bf16x8 ld128(LAS char* p) { return *(LAS bf16x8*)p; }
#define MFMA16(a, b, c) __builtin_amdgcn_mfma_f32_16x16x32_bf16((a), (b), (c), 0, 0, 0)
#define MFMA32(a, b, c) __builtin_amdgcn_mfma_f32_32x32x16_bf16((a), (b), (c), 0, 0, 0)
#define WAITV(n) asm volatile("s_waitcnt vmcnt(" #n ")" ::: "memory")
#define WAITL(n) asm volatile("s_waitcnt lgkmcnt(" #n ")" ::: "memory")
#define WAITL0() __builtin_amdgcn_s_waitcnt(0xC07F)
#define BAR() __builtin_amdgcn_s_barrier()

DI float gelu_t(float v) {
  const float c1 = -2.302208198f, c2 = -0.1029432397f;
  float z = v * (c1 + c2 * v * v);
  float e = __builtin_amdgcn_exp2f(z);
  return v * __builtin_amdgcn_rcpf(1.f + e);
}

#define STAMP(k, i)
__global__ __launch_bounds__(256) void k_fwdS(const u16* __restrict__ x, const u16* __restrict__ gw, u16* __restrict__ T, float* __restrict__ g) {
  extern __shared__ __attribute__((aligned(16))) char smem[];
  LAS char* lds = (LAS char*)smem;
  const int tid = threadIdx.x, lane = tid & 63, wid = __builtin_amdgcn_readfirstlane(tid >> 6);
  const int b = blockIdx.x;
  const int beta = b & 7, dblk = (b >> 3) & 15, dh = b >> 7;
  STAMP(0, 0);
  rsrc_t rx = mkrs(x + beta * D_ + dblk * 64, (unsigned)(S_ * D_ * 2 - (beta * D_ + dblk * 64) * 2));
  {
    const int hi = lane >> 5, r = (lane & 31) >> 2, c = (lane & 3) ^ (2 * ((r >> 2) & 1));
    const unsigned voff = r * 16384 + hi * 64 + c * 16;
#pragma unroll
    for (int i = 0; i < 8; i++) { const int j = wid * 8 + i; dma16(rx, lds + j * 1024, voff, j * 8 * 16384); }
  }
  bf16x8 ef[2][8];
  {
    const u32x4* et = (const u32x4*)&ETAB1;
#pragma unroll
    for (int c = 0; c < 2; c++)
#pragma unroll
      for (int kb = 0; kb < 8; kb++) ef[c][kb] = __builtin_bit_cast(bf16x8, et[((dh * 8 + wid * 2 + c) * 8 + kb) * 64 + lane]);
  }
  {
    const u32x4* gp = (const u32x4*)gw;
    u32x4 g0 = gp[lane * 2], g1 = gp[lane * 2 + 1];
#pragma unroll
    for (int rr = 0; rr < 2; rr++) {
      const int s = b * 8 + wid * 2 + rr;
      const u32x4* xp = (const u32x4*)(x + s * D_);
      u32x4 x0 = xp[lane * 2], x1 = xp[lane * 2 + 1];
      float acc = 0.f;
#pragma unroll
      for (int e = 0; e < 4; e++) {
        acc += rbf(__uint_as_float(x0[e] << 16) * __uint_as_float(g0[e] << 16));
        acc += rbf(__uint_as_float(x0[e] & 0xffff0000u) * __uint_as_float(g0[e] & 0xffff0000u));
        acc += rbf(__uint_as_float(x1[e] << 16) * __uint_as_float(g1[e] << 16));
        acc += rbf(__uint_as_float(x1[e] & 0xffff0000u) * __uint_as_float(g1[e] & 0xffff0000u));
      }
#pragma unroll
      for (int o = 32; o > 0; o >>= 1) acc += __shfl_xor(acc, o);
      if (lane == 0) { float z = rbf(acc); g[256 * (s & 7) + (s >> 3)] = rbf(1.f / (1.f + __expf(-z))); }
    }
  }
  WAITV(0); BAR();
  STAMP(0, 1);
  const int gq = lane >> 4, q = (lane & 15) >> 2, p = lane & 3;
  f32x4 acc[2][4];
#pragma unroll
  for (int c = 0; c < 2; c++)
#pragma unroll
    for (int i = 0; i < 4; i++) acc[c][i] = (f32x4){0.f, 0.f, 0.f, 0.f};
  LAS char* base = lds + (gq >> 1) * 2 * 512 + (4 * (gq & 1) + q) * 64 + ((p ^ (2 * (gq & 1))) * 16);
#pragma unroll
  for (int kb = 0; kb < 8; kb++) {
#pragma unroll
    for (int i = 0; i < 4; i++) {
      LAS char* a = base + (4 * kb) * 1024 + (i >> 1) * 512 + (i & 1) * 8;
      bf16x8 A = cat8(trd(a), trd(a + 2 * 1024));
#pragma unroll
      for (int c = 0; c < 2; c++) acc[c][i] = MFMA16(A, ef[c][kb], acc[c][i]);
    }
  }
#pragma unroll
  for (int c = 0; c < 2; c++) {
    const int dp = dh * 128 + wid * 32 + c * 16 + (lane & 15);
#pragma unroll
    for (int ip = 0; ip < 2; ip++) {
      u32x4 w;
      w[0] = pk2(acc[c][2 * ip][0], acc[c][2 * ip][1]); w[1] = pk2(acc[c][2 * ip][2], acc[c][2 * ip][3]);
      w[2] = pk2(acc[c][2 * ip + 1][0], acc[c][2 * ip + 1][1]); w[3] = pk2(acc[c][2 * ip + 1][2], acc[c][2 * ip + 1][3]);
      *(u32x4*)(T + (beta * 256 + dp) * D_ + dblk * 64 + ip * 32 + gq * 8) = w;
    }
  }
  STAMP(0, 2); STAMP(0, 3);
}

#define SB() __builtin_amdgcn_sched_barrier(0)
constexpr int G1_XB = 16384, G1_WB = 32768, G1_W0 = 3 * G1_XB, G1_BI = G1_W0 + 3 * G1_WB, G1_LDS = G1_BI + 1024;
__global__ __launch_bounds__(512, 2) void k_gemm1(const u16* __restrict__ x, const u16* __restrict__ w1, const u16* __restrict__ b1, u16* __restrict__ h) {
  extern __shared__ __attribute__((aligned(16))) char smem[];
  LAS char* lds = (LAS char*)smem;
  const int tid = threadIdx.x, lane = tid & 63, wid = __builtin_amdgcn_readfirstlane(tid >> 6);
  const int b = blockIdx.x, xcd = b & 7, idx = b >> 3;
  const int mb = (xcd & 1) * 8 + (idx & 7), nb = (xcd >> 1) * 4 + (idx >> 3);
  const int beta = mb >> 1, alpha0 = (mb & 1) * 128;
  STAMP(1, 0);
  if (wid >= 4) {
    const int lw = wid - 4;
    const unsigned xo = (8 * alpha0 + beta) * D_;
    const unsigned wo = (nb * 448) & ~127u;
    rsrc_t rx = mkrs(x + xo, (S_ * D_ - xo) * 2);
    rsrc_t rw = mkrs((const char*)w1 + wo, D_ * F_ * 2 - wo);
    rsrc_t rb = mkrs(b1 + nb * 224, 448);
    unsigned vx[2], vw[8];
    { const int r = lane >> 3, c1 = lane & 7;
      vx[0] = r * 16384 + ((c1 ^ (r >> 1)) * 16); vx[1] = r * 16384 + ((c1 ^ (4 + (r >> 1))) * 16); }
#pragma unroll
    for (int i = 0; i < 8; i++) { const int hi = lane >> 5, k = 2 * i + hi, f = (k & 3) | (((k >> 3) & 1) << 2);
      vw[i] = hi * (F_ * 2) + (((lane & 31) ^ (f << 1)) * 16); }
    auto stage = [&](int kt, int buf) {
#pragma unroll
      for (int i = 0; i < 4; i++) { const int j = lw * 4 + i; dma16(rx, lds + buf * G1_XB + j * 1024, vx[i & 1], j * 8 * 16384 + kt * 128); }
#pragma unroll
      for (int i = 0; i < 8; i++) { const int j = lw * 8 + i; dma16(rw, lds + G1_W0 + buf * G1_WB + j * 1024, vw[i], (kt * 64 + 2 * j) * (F_ * 2)); }
    };
    if (lw == 0) dma16(rb, lds + G1_BI, lane * 16, 0);
    stage(0, 0); stage(1, 1); stage(2, 2);
    WAITV(24); BAR();
    int cb = 0;
    for (int kt = 0; kt < 15; kt++) {
      if (kt < 14) WAITV(12); else WAITV(0);
      BAR();
      if (kt + 3 < 16) stage(kt + 3, cb);
      cb = (cb == 2) ? 0 : cb + 1;
    }
    return;
  }
  const int wm = wid >> 1, wn = wid & 1;
  const int c = lane & 15, gq = lane >> 4, q = (lane & 15) >> 2, p = lane & 3;
  LAS char* xa0 = lds + (64 * wm + c) * 128 + ((gq ^ (c >> 1)) * 16);
  LAS char* xa1 = lds + (((64 * wm + c) * 128 + ((gq ^ (c >> 1)) * 16)) ^ 64);
  const unsigned wa0 = G1_W0 + (8 * gq + q) * 512 + ((q | ((gq & 1) << 2)) << 5) + ((p >> 1) << 4) + ((p & 1) << 3);
  const unsigned tsh = (7 * wn + 2 * (nb & 1));
  unsigned wa[7];
#pragma unroll
  for (int t = 0; t < 7; t++) wa[t] = wa0 ^ ((tsh + t) << 5);
  f32x4 acc[7][4];
  bf16x8 xf[2][4];
  s16x4 wl[2][7], wh[2][7];
  BAR();
  STAMP(1, 1);
#pragma unroll
  for (int t = 0; t < 7; t++) { u32x2 bv = *(LAS u32x2*)(lds + G1_BI + (16 * (7 * wn + t) + 4 * gq) * 2);
    f32x4 bb = {__uint_as_float(bv[0] << 16), __uint_as_float(bv[0] & 0xffff0000u), __uint_as_float(bv[1] << 16), __uint_as_float(bv[1] & 0xffff0000u)};
#pragma unroll
    for (int i = 0; i < 4; i++) acc[t][i] = bb; }
#define RDX(set, buf, ks, i) xf[set][i] = ld128(((ks) ? xa1 : xa0) + (buf) * G1_XB + (i) * 2048)
#define RDWL(set, buf, ks, t) wl[set][t] = trd(lds + wa[t] + ((buf) & 1) * G1_WB + (ks) * 16384)
#define RDWH(set, buf, ks, t) wh[set][t] = trd(lds + wa[t] + ((buf) & 1) * G1_WB + (ks) * 16384 + 2048)
#define MM(set, t, i) acc[t][i] = MFMA16(cat8(wl[set][t], wh[set][t]), xf[set][i], acc[t][i])
#define RDS(ns, buf, ks, n) do { if ((n) < 4) RDX(ns, buf, ks, (n) & 3); else if ((n) < 18) { if (((n) & 1) == 0) RDWL(ns, buf, ks, (((n) - 4) >> 1) % 7); else RDWH(ns, buf, ks, (((n) - 4) >> 1) % 7); } } while (0)
#define PHASE(cs, ns, buf, ks, DO_RD, ADJ) do { \
    _Pragma("unroll") for (int s_ = 0; s_ < 28; s_++) { MM(cs, s_ >> 2, s_ & 3); if (DO_RD) RDS(ns, buf, ks, s_); \
      if ((ADJ) != 0 && s_ >= 18 && s_ < 25) { wa[(s_ - 18) % 7] += (ADJ); asm volatile("" : "+v"(wa[(s_ - 18) % 7])); } SB(); } \
    WAITL0(); } while (0)
#pragma unroll
  for (int n = 0; n < 18; n++) RDS(0, 0, 0, n);
  WAITL0();
  for (int k3 = 0; k3 < 5; k3++) {
    PHASE(0, 1, 0, 1, true, 0); BAR(); SB(); PHASE(1, 0, 1, 0, true, 0);
    PHASE(0, 1, 1, 1, true, 2 * G1_WB); BAR(); SB(); PHASE(1, 0, 2, 0, true, 0);
    PHASE(0, 1, 2, 1, true, -2 * G1_WB); BAR(); SB(); PHASE(1, 0, 0, 0, true, 0);
  }
  PHASE(0, 1, 0, 1, true, 0);
  PHASE(1, 0, 0, 0, false, 0);
  STAMP(1, 2);
#pragma unroll
  for (int i = 0; i < 4; i++) {
    char* hb = (char*)h + ((size_t)(8 * mb + 4 * wm + i) * 112 + 7 * nb) * 1024 + lane * 16;
    unsigned pk[7][2];
#pragma unroll
    for (int t = 0; t < 7; t++) { pk[t][0] = pk2(gelu_t(acc[t][i][0]), gelu_t(acc[t][i][1])); pk[t][1] = pk2(gelu_t(acc[t][i][2]), gelu_t(acc[t][i][3])); }
    if (wn == 0) {
#pragma unroll
      for (int pr = 0; pr < 3; pr++) *(u32x4*)(hb + pr * 1024) = (u32x4){pk[2 * pr][0], pk[2 * pr][1], pk[2 * pr + 1][0], pk[2 * pr + 1][1]};
      *(u32x2*)(hb + 3 * 1024) = (u32x2){pk[6][0], pk[6][1]};
    } else {
      *(u32x2*)(hb + 3 * 1024 + 8) = (u32x2){pk[0][0], pk[0][1]};
#pragma unroll
      for (int pr = 0; pr < 3; pr++) *(u32x4*)(hb + (4 + pr) * 1024) = (u32x4){pk[2 * pr + 1][0], pk[2 * pr + 1][1], pk[2 * pr + 2][0], pk[2 * pr + 2][1]};
    }
  }
  STAMP(1, 3);
}

constexpr int PS = 2080;
constexpr int KB_LDS = 16 * PS;
DI int crow(int r, int h) { return (r & 3) + 8 * (r >> 2) + 4 * h; }
DI bf16x8 pack8(const f32x16& v, int s) { u32x4 w; w[0] = pk2(v[8 * s], v[8 * s + 1]); w[1] = pk2(v[8 * s + 2], v[8 * s + 3]); w[2] = pk2(v[8 * s + 4], v[8 * s + 5]); w[3] = pk2(v[8 * s + 6], v[8 * s + 7]); return __builtin_bit_cast(bf16x8, w); }
__global__ __launch_bounds__(256) void k_spec(const u16* __restrict__ T, const u16* __restrict__ wr, const u16* __restrict__ wi, u16* __restrict__ T2) {
  extern __shared__ __attribute__((aligned(16))) char smem[];
  LAS char* lds = (LAS char*)smem;
  const int tid = threadIdx.x, lane = tid & 63, wid = __builtin_amdgcn_readfirstlane(tid >> 6);
  const int dl = blockIdx.x;
  STAMP(2, 0);
  const bool realu = (dl == 0) || (dl == 128);
  const int pre = (dl == 0) ? 0 : (dl == 128 ? 1 : 2 * dl), pim = realu ? pre : pre + 1;
  rsrc_t rt = mkrs(T, 8 * 256 * 1024 * 2);
#pragma unroll
  for (int i = 0; i < 8; i++) { const int j = wid * 8 + i, pl = j >> 1, hf = j & 1, beta = pl >> 1, ri = pl & 1;
    dma16(rt, lds + pl * PS + hf * 1024, lane * 16, ((beta * 256 + (ri ? pim : pre)) * 1024) * 2 + hf * 1024); }
  s16x4 Mf, Mi;
  { const int n = lane & 15, g = lane >> 4; const float sc = (realu ? 1.f : 2.f) * (1.f / 2097152.f);
    float f[4], iv[4];
#pragma unroll
    for (int j = 0; j < 4; j++) { const int k = 4 * g + j;
      { const int be = k >> 1, ri = k & 1, ga = n >> 1, ro = n & 1; const int jj = dl + 256 * ga; const float fr = (float)((be * jj) & 2047) * (1.f / 2048.f);
        const float cs = __builtin_amdgcn_cosf(fr), sn = __builtin_amdgcn_sinf(fr);
        float v = (ri == ro) ? cs : (ri ? sn : -sn); if (realu && ri) v = 0.f; f[j] = v; }
      { const int ga = k >> 1, ri = k & 1, be = n >> 1, ro = n & 1; const int jj = dl + 256 * ga; const float fr = (float)((be * jj) & 2047) * (1.f / 2048.f);
        const float cs = __builtin_amdgcn_cosf(fr), sn = __builtin_amdgcn_sinf(fr);
        iv[j] = sc * ((ri == ro) ? cs : (ri ? -sn : sn)); } }
    u32x2 a = {pk2(f[0], f[1]), pk2(f[2], f[3])}, bb = {pk2(iv[0], iv[1]), pk2(iv[2], iv[3])};
    Mf = __builtin_bit_cast(s16x4, a); Mi = __builtin_bit_cast(s16x4, bb); }
  const int l31 = lane & 31, hh = lane >> 5;
  bf16x8 Cc[2], Sn[2], nS[2];
#pragma unroll
  for (int s = 0; s < 2; s++) { u32x4 c4, s4, n4;
#pragma unroll
    for (int e = 0; e < 4; e++) { const int k0 = crow(8 * s + 2 * e, hh), k1 = crow(8 * s + 2 * e + 1, hh);
      const float f0 = (float)((l31 * k0) & 31) * (1.f / 32.f), f1 = (float)((l31 * k1) & 31) * (1.f / 32.f);
      const float c0 = __builtin_amdgcn_cosf(f0), c1 = __builtin_amdgcn_cosf(f1), s0 = __builtin_amdgcn_sinf(f0), s1 = __builtin_amdgcn_sinf(f1);
      c4[e] = pk2(c0, c1); s4[e] = pk2(s0, s1); n4[e] = pk2(-s0, -s1); }
    Cc[s] = __builtin_bit_cast(bf16x8, c4); Sn[s] = __builtin_bit_cast(bf16x8, s4); nS[s] = __builtin_bit_cast(bf16x8, n4); }
  float twc[16], tws[16];
#pragma unroll
  for (int r = 0; r < 16; r++) { const float fr = (float)(crow(r, hh) * l31) * (1.f / 1024.f); twc[r] = __builtin_amdgcn_cosf(fr); tws[r] = __builtin_amdgcn_sinf(fr); }
  WAITV(0); BAR();
  STAMP(2, 1);
  { const int g = lane >> 4, q = (lane & 15) >> 2, p = lane & 3, n = lane & 15;
#pragma unroll 4
    for (int t = 0; t < 16; t++) { const int d0 = wid * 256 + t * 16;
      s16x4 a = trd(lds + (4 * g + q) * PS + (d0 + 4 * p) * 2);
      f32x4 dd = __builtin_amdgcn_mfma_f32_16x16x16bf16_1k(a, Mf, (f32x4){0.f, 0.f, 0.f, 0.f}, 0, 0, 0);
      *(LAS u32x2*)(lds + n * PS + (d0 + 4 * g) * 2) = (u32x2){pk2(dd[0], dd[1]), pk2(dd[2], dd[3])}; } }
  WAITL(0); BAR();
#pragma unroll 1
  for (int rr = 0; rr < 2; rr++) {
    const int ga = wid * 2 + rr, j = dl + 256 * ga, jm = (2048 - j) & 2047;
    float fr_[16], fi_[16];
#pragma unroll
    for (int r = 0; r < 16; r++) { const int k = 32 * crow(r, hh) + l31;
      if (r < 8) { fr_[r] = bf2f(wr[j * DF_ + k]); fi_[r] = bf2f(wi[j * DF_ + k]); }
      else { fr_[r] = bf2f(wr[jm * DF_ + 1024 - k]); fi_[r] = -bf2f(wi[jm * DF_ + 1024 - k]); } }
    if (lane == 0) {
      fr_[0] = 0.5f * (fr_[0] + bf2f(wr[jm * DF_])); fi_[0] = 0.5f * (fi_[0] - bf2f(wi[jm * DF_]));
      fr_[8] = 0.5f * (fr_[8] + bf2f(wr[j * DF_ + 512])); fi_[8] = 0.5f * (fi_[8] + bf2f(wi[j * DF_ + 512])); }
    LAS char* pr = lds + (2 * ga) * PS; LAS char* pi = pr + PS;
    const int blk = (lane >> 4) & 1, q = (lane & 15) >> 2, p = lane & 3;
    bf16x8 Xr[2], Xi[2];
#pragma unroll
    for (int s = 0; s < 2; s++) { const int off = (16 * s + 4 * hh + q) * 64 + (16 * blk + 4 * p) * 2;
      Xr[s] = cat8(trd(pr + off), trd(pr + off + 512)); Xi[s] = cat8(trd(pi + off), trd(pi + off + 512)); }
    f32x16 re, im; const f32x16 z = {0.f};
    re = MFMA32(Xr[0], Cc[0], z); re = MFMA32(Xr[1], Cc[1], re); re = MFMA32(Xi[0], Sn[0], re); re = MFMA32(Xi[1], Sn[1], re);
    im = MFMA32(Xi[0], Cc[0], z); im = MFMA32(Xi[1], Cc[1], im); im = MFMA32(Xr[0], nS[0], im); im = MFMA32(Xr[1], nS[1], im);
#pragma unroll
    for (int r = 0; r < 16; r++) { const float a = re[r], b = im[r]; re[r] = a * twc[r] + b * tws[r]; im[r] = b * twc[r] - a * tws[r]; }
#pragma unroll
    for (int s = 0; s < 2; s++) { Xr[s] = pack8(re, s); Xi[s] = pack8(im, s); }
    re = MFMA32(Cc[0], Xr[0], z); re = MFMA32(Cc[1], Xr[1], re); re = MFMA32(Sn[0], Xi[0], re); re = MFMA32(Sn[1], Xi[1], re);
    im = MFMA32(Cc[0], Xi[0], z); im = MFMA32(Cc[1], Xi[1], im); im = MFMA32(nS[0], Xr[0], im); im = MFMA32(nS[1], Xr[1], im);
#pragma unroll
    for (int r = 0; r < 16; r++) { const float a = re[r], b = im[r]; re[r] = a * fr_[r] - b * fi_[r]; im[r] = a * fi_[r] + b * fr_[r]; }
#pragma unroll
    for (int s = 0; s < 2; s++) { Xr[s] = pack8(re, s); Xi[s] = pack8(im, s); }
    re = MFMA32(Xr[0], Cc[0], z); re = MFMA32(Xr[1], Cc[1], re); re = MFMA32(Xi[0], nS[0], re); re = MFMA32(Xi[1], nS[1], re);
    im = MFMA32(Xi[0], Cc[0], z); im = MFMA32(Xi[1], Cc[1], im); im = MFMA32(Xr[0], Sn[0], im); im = MFMA32(Xr[1], Sn[1], im);
#pragma unroll
    for (int r = 0; r < 16; r++) { const float a = re[r], b = im[r]; re[r] = a * twc[r] - b * tws[r]; im[r] = b * twc[r] + a * tws[r]; }
#pragma unroll
    for (int s = 0; s < 2; s++) { Xr[s] = pack8(re, s); Xi[s] = pack8(im, s); }
    re = MFMA32(Xr[0], Cc[0], z); re = MFMA32(Xr[1], Cc[1], re); re = MFMA32(Xi[0], nS[0], re); re = MFMA32(Xi[1], nS[1], re);
    im = MFMA32(Xi[0], Cc[0], z); im = MFMA32(Xi[1], Cc[1], im); im = MFMA32(Xr[0], Sn[0], im); im = MFMA32(Xr[1], Sn[1], im);
#pragma unroll
    for (int G = 0; G < 4; G++) { const int off = (32 * l31 + 8 * G + 4 * hh) * 2;
      *(LAS u32x2*)(pr + off) = (u32x2){pk2(re[4 * G], re[4 * G + 1]), pk2(re[4 * G + 2], re[4 * G + 3])};
      *(LAS u32x2*)(pi + off) = (u32x2){pk2(im[4 * G], im[4 * G + 1]), pk2(im[4 * G + 2], im[4 * G + 3])}; }
  }
  WAITL(0); BAR();
  STAMP(2, 2);
  { const int g = lane >> 4, q = (lane & 15) >> 2, p = lane & 3, n = lane & 15, be = n >> 1, ro = n & 1;
    u16* dst = T2 + (be * 256 + (ro ? pim : pre)) * 1024;
#pragma unroll 2
    for (int t = 0; t < 8; t++) { const int d0 = wid * 256 + t * 32;
      s16x4 a0 = trd(lds + (4 * g + q) * PS + (d0 + 8 * p) * 2), a1 = trd(lds + (4 * g + q) * PS + (d0 + 8 * p + 4) * 2);
      f32x4 d0v = __builtin_amdgcn_mfma_f32_16x16x16bf16_1k(a0, Mi, (f32x4){0.f, 0.f, 0.f, 0.f}, 0, 0, 0);
      f32x4 d1v = __builtin_amdgcn_mfma_f32_16x16x16bf16_1k(a1, Mi, (f32x4){0.f, 0.f, 0.f, 0.f}, 0, 0, 0);
      if (!(realu && ro)) *(u32x4*)(dst + d0 + 8 * g) = (u32x4){pk2(d0v[0], d0v[1]), pk2(d0v[2], d0v[3]), pk2(d1v[0], d1v[1]), pk2(d1v[2], d1v[3])}; } }
  STAMP(2, 3);
}

constexpr int G2_HB = 16384, G2_WB = 8192, G2_ST = G2_HB + G2_WB, G2_NST = 5, G2_LDS = G2_NST * G2_ST;
__global__ __launch_bounds__(256, 1) void k_gemm2(const u16* __restrict__ h, const u16* __restrict__ w2, const u16* __restrict__ b2, const u16* __restrict__ T2,
                                                  const float* __restrict__ g, u16* __restrict__ out) {
  extern __shared__ __attribute__((aligned(16))) char smem[];
  LAS char* lds = (LAS char*)smem;
  const int tid = threadIdx.x, lane = tid & 63, wid = __builtin_amdgcn_readfirstlane(tid >> 6);
  const int b = blockIdx.x, xcd = b & 7, idx = b >> 3;
  const int mb = (xcd >> 1) * 4 + (idx & 3), nb = (xcd & 1) * 8 + (idx >> 2);
  const int beta = mb >> 1, alpha0 = (mb & 1) * 128, d0 = nb * 64;
  const int wm = wid >> 1, wn = wid & 1;
  rsrc_t rh = mkrs((const char*)h + (size_t)mb * 8 * 112 * 1024, 8 * 112 * 1024);
  rsrc_t rw = mkrs(w2 + d0, (F_ * D_ - d0) * 2);
  rsrc_t re = mkrs((const char*)&ETAB2 + (mb & 1) * 8 * 8 * 1024, 8 * 8 * 1024);
  rsrc_t rt = mkrs(T2 + beta * 256 * 1024 + d0, (256 * 1024 - d0) * 2);
  const unsigned vh = lane * 16;
  unsigned vw; { const int hi = lane >> 5, r = (lane & 31) >> 2, c = (lane & 3) ^ (2 * ((r >> 2) & 1)); vw = r * 2048 + hi * 64 + c * 16; }
  auto stage = [&](int kt, int buf) {
    LAS char* sb = lds + buf * G2_ST;
    if (kt < 56) {
#pragma unroll
      for (int i = 0; i < 4; i++) { const int j = wid * 4 + i; dma16(rh, sb + j * 1024, vh, ((j >> 1) * 112 + 2 * kt + (j & 1)) * 1024); }
#pragma unroll
      for (int i = 0; i < 2; i++) { const int j = wid * 2 + i; dma16(rw, sb + G2_HB + j * 1024, vw, (kt * 64 + j * 8) * 2048); }
    } else {
      const int k2 = kt - 56;
#pragma unroll
      for (int i = 0; i < 4; i++) { const int j = wid * 4 + i; dma16(re, sb + j * 1024, vh, ((j >> 1) * 8 + 2 * k2 + (j & 1)) * 1024); }
#pragma unroll
      for (int i = 0; i < 2; i++) { const int j = wid * 2 + i; dma16(rt, sb + G2_HB + j * 1024, vw, (k2 * 64 + j * 8) * 2048); }
    }
  };
  constexpr int NT = 60;
  STAMP(3, 0);
#pragma unroll
  for (int i = 0; i < G2_NST; i++) stage(i, i);
  const int gq = lane >> 4, q = (lane & 15) >> 2, p = lane & 3;
  const unsigned hb = (4 * wm) * 2048 + lane * 16;
  const unsigned wb0 = G2_HB + (gq >> 1) * 1024 + wn * 512 + (4 * (gq & 1) + q) * 64 + (((p >> 1) ^ (2 * (gq & 1))) * 16) + 8 * (p & 1), wb1 = wb0 ^ 32;
  f32x4 acc[2][4], accb[2][4];
#pragma unroll
  for (int t = 0; t < 2; t++)
#pragma unroll
    for (int i = 0; i < 4; i++) { acc[t][i] = (f32x4){0.f, 0.f, 0.f, 0.f}; accb[t][i] = acc[t][i]; }
  bf16x8 hf[2][4], wf[2][2];
  auto rd = [&](int set, int ks, unsigned boff) {
#pragma unroll
    for (int i = 0; i < 4; i++) hf[set][i] = ld128(lds + boff + hb + i * 2048 + ks * 1024);
#pragma unroll
    for (int t = 0; t < 2; t++) { LAS char* a = lds + boff + (t ? wb1 : wb0) + ks * 4096; wf[set][t] = cat8(trd(a), trd(a + 2048)); } };
  auto mma = [&](int set) {
#pragma unroll
    for (int t = 0; t < 2; t++)
#pragma unroll
      for (int i = 0; i < 4; i++) acc[t][i] = MFMA16(wf[set][t], hf[set][i], acc[t][i]); };
  WAITV(24); BAR();
  STAMP(3, 1);
  rd(0, 0, 0);
  unsigned cur = 0; int cb = 0;
  for (int kt = 0; kt < NT; kt++) {
    rd(1, 1, cur);
    mma(0);
    WAITL(0);
    { const int rem = NT - 1 - kt;
      if (rem >= 4) WAITV(18); else if (rem == 3) WAITV(12); else if (rem == 2) WAITV(6); else WAITV(0); }
    BAR();
    if (kt + G2_NST < NT) stage(kt + G2_NST, cb);
    unsigned nxt = cur + G2_ST; int nb2 = cb + 1; if (nb2 == G2_NST) { nb2 = 0; nxt = 0; }
    if (kt < NT - 1) rd(0, 0, nxt);
    mma(1);
    if (kt == 55) {
#pragma unroll
      for (int t = 0; t < 2; t++)
#pragma unroll
        for (int i = 0; i < 4; i++) { accb[t][i] = acc[t][i]; acc[t][i] = (f32x4){0.f, 0.f, 0.f, 0.f}; }
    }
    cur = nxt; cb = nb2;
  }
  STAMP(3, 2);
#pragma unroll
  for (int t = 0; t < 2; t++) {
    const int dd = d0 + 32 * wn + 16 * t + 4 * gq;
    const u32x2 bv = *(const u32x2*)(b2 + dd);
    const float bb[4] = {__uint_as_float(bv[0] << 16), __uint_as_float(bv[0] & 0xffff0000u), __uint_as_float(bv[1] << 16), __uint_as_float(bv[1] & 0xffff0000u)};
#pragma unroll
    for (int i = 0; i < 4; i++) {
      const int al = alpha0 + 64 * wm + 16 * i + (lane & 15);
      const float gg = g[256 * beta + al], og = rbf(1.f - gg);
      float o[4];
#pragma unroll
      for (int r = 0; r < 4; r++) { const float bval = rbf(rbf(accb[t][i][r]) + bb[r]), a = rbf(acc[t][i][r]); o[r] = rbf(gg * a) + rbf(og * bval); }
      *(u32x2*)(out + (8 * al + beta) * D_ + dd) = (u32x2){pk2(o[0], o[1]), pk2(o[2], o[3])};
    }
  }
  STAMP(3, 3);
}

extern "C" void kernel_launch(void* const* d_in, const int* in_sizes, int n_in, void* d_out, int out_size, void* d_ws, size_t ws_size, hipStream_t stream) {
  const u16* x = (const u16*)d_in[0]; const u16* wr = (const u16*)d_in[1]; const u16* wi = (const u16*)d_in[2];
  const u16* w1 = (const u16*)d_in[4]; const u16* b1 = (const u16*)d_in[5]; const u16* w2 = (const u16*)d_in[6]; const u16* b2 = (const u16*)d_in[7];
  const u16* gw = (const u16*)d_in[8];
  char* ws = (char*)d_ws;
  float* g = (float*)ws;
  u16* T = (u16*)(ws + (1 << 20));
  u16* T2 = (u16*)(ws + (5 << 20));
  u16* h = (u16*)(ws + (9 << 20));
  hipFuncSetAttribute((const void*)k_gemm1, hipFuncAttributeMaxDynamicSharedMemorySize, G1_LDS);
  hipFuncSetAttribute((const void*)k_gemm2, hipFuncAttributeMaxDynamicSharedMemorySize, G2_LDS);
  k_fwdS<<<256, 256, 32768, stream>>>(x, gw, T, g);
  k_gemm1<<<256, 512, G1_LDS, stream>>>(x, w1, b1, h);
  k_spec<<<129, 256, KB_LDS, stream>>>(T, wr, wi, T2);
  k_gemm2<<<256, 256, G2_LDS, stream>>>(h, w2, b2, T2, g, (u16*)d_out);
}
```

```cpp
#include <hip/hip_runtime.h>
#include <stdint.h>
typedef unsigned short u16;
#define LAS __attribute__((address_space(3)))
#define DI __device__ __forceinline__
typedef short s16x4 __attribute__((ext_vector_type(4)));
typedef short s16x8 __attribute__((ext_vector_type(8)));
typedef __bf16 bf16x8 __attribute__((ext_vector_type(8)));
typedef __bf16 bf16x2 __attribute__((ext_vector_type(2)));
typedef float f32x2 __attribute__((ext_vector_type(2)));
typedef float f32x4 __attribute__((ext_vector_type(4)));
typedef float f32x16 __attribute__((ext_vector_type(16)));
typedef unsigned u32x2 __attribute__((ext_vector_type(2)));
typedef unsigned u32x4 __attribute__((ext_vector_type(4)));
typedef __amdgpu_buffer_rsrc_t rsrc_t;

constexpr int S_ = 2048, D_ = 1024, F_ = 3584, DF_ = 513;

namespace ct {
constexpr double PI = 3.14159265358979323846;
constexpr double ccos(double x) { double x2 = x * x, t = 1, s = 1; for (int i = 1; i < 16; i++) { t *= -x2 / ((2 * i - 1) * (2 * i)); s += t; } return s; }
constexpr double cos256(int m) { m &= 255; if (m > 128) m = 256 - m; bool neg = false; if (m > 64) { m = 128 - m; neg = true; } double v = (m == 64) ? 0.0 : ccos(2 * PI * m / 256.0); return neg ? -v : v; }
constexpr u16 tobf(double d) { float f = (float)d; unsigned u = __builtin_bit_cast(unsigned, f); u += 0x7fffu + ((u >> 16) & 1u); return (u16)(u >> 16); }
struct Tab { u16 c[256]; };
constexpr Tab mktab() { Tab t{}; for (int i = 0; i < 256; i++) t.c[i] = tobf(cos256(i)); return t; }
constexpr Tab COS = mktab();
constexpr u16 E(int dp, int a) { if (dp == 0) return COS.c[0]; if (dp == 1) return COS.c[(128 * a) & 255];
  int dl = dp >> 1; int m = (dl * a) & 255; return (dp & 1) ? COS.c[(m + 64) & 255] : COS.c[m]; }
struct Chunk { u16 v[8192]; };
template <bool TR> constexpr Chunk mk(int ch) { Chunk c{}; for (int i = 0; i < 8192; i++) { int j = i & 7, lane = (i >> 3) & 63, kb = (i >> 9) & 7, blk = ch * 2 + (i >> 12);
  int col = blk * 16 + (lane & 15); int k = 32 * kb + 16 * (j >> 2) + 4 * (lane >> 4) + (j & 3); c.v[i] = TR ? E(k, col) : E(col, k); } return c; }
template <bool TR, int I> constexpr Chunk CH = mk<TR>(I);
struct Big { Chunk c[8]; };
}
__device__ __attribute__((aligned(1024))) const ct::Big ETAB1 = {{ct::CH<false,0>, ct::CH<false,1>, ct::CH<false,2>, ct::CH<false,3>, ct::CH<false,4>, ct::CH<false,5>, ct::CH<false,6>, ct::CH<false,7>}};
__device__ __attribute__((aligned(1024))) const ct::Big ETAB2 = {{ct::CH<true,0>, ct::CH<true,1>, ct::CH<true,2>, ct::CH<true,3>, ct::CH<true,4>, ct::CH<true,5>, ct::CH<true,6>, ct::CH<true,7>}};

namespace ct {
constexpr double cosq(int m, int n) {
  m %= n; if (m < 0) m += n; if (2 * m > n) m = n - m; bool neg = false; if (4 * m > n) { m = n / 2 - m; neg = true; }
  double v = (4 * m == n) ? 0.0 : ((8 * m > n) ? 0.0 : 0.0);
  if (4 * m != n) { double x = 2 * PI * m / n; v = ccos(x); }
  return neg ? -v : v; }
struct C2048 { double c[513]; };
constexpr C2048 mk2048() { C2048 t{}; for (int i = 0; i <= 512; i++) t.c[i] = (i == 512) ? 0.0 : ccos(2 * PI * i / 2048.0); return t; }
constexpr C2048 CQ = mk2048();
constexpr double c2048(int m) { m &= 2047; if (m > 1024) m = 2048 - m; return m > 512 ? -CQ.c[1024 - m] : CQ.c[m]; }
constexpr double s2048(int m) { return c2048(m - 512); }
constexpr unsigned f2u(double d) { return __builtin_bit_cast(unsigned, (float)d); }
constexpr int crow_(int r, int h) { return (r & 3) + 8 * (r >> 2) + 4 * h; }
struct CTab { unsigned v[12 * 64 * 4]; };
constexpr CTab mkctab() { CTab t{};
  for (int lane = 0; lane < 64; lane++) { const int l31 = lane & 31, hh = lane >> 5;
    for (int s = 0; s < 2; s++) for (int e = 0; e < 4; e++) { const int k0 = crow_(8 * s + 2 * e, hh), k1 = crow_(8 * s + 2 * e + 1, hh);
      t.v[((0 + s) * 64 + lane) * 4 + e] = (unsigned)tobf(c2048(64 * l31 * k0)) | ((unsigned)tobf(c2048(64 * l31 * k1)) << 16);
      t.v[((2 + s) * 64 + lane) * 4 + e] = (unsigned)tobf(s2048(64 * l31 * k0)) | ((unsigned)tobf(s2048(64 * l31 * k1)) << 16); }
    for (int r = 0; r < 16; r++) { t.v[((4 + (r >> 2)) * 64 + lane) * 4 + (r & 3)] = f2u(c2048(2 * crow_(r, hh) * l31)); t.v[((8 + (r >> 2)) * 64 + lane) * 4 + (r & 3)] = f2u(s2048(2 * crow_(r, hh) * l31)); } }
  return t; }
struct MChunk { unsigned v[8 * 64 * 4]; };
constexpr int jp_(bool sp, int dl, int m) { return sp ? (m < 2 ? 1024 * m : (m < 8 ? 256 * (m >> 1) : 128 + 256 * ((m - 8) >> 1))) : dl + 256 * (m >> 1); }
constexpr MChunk mkm(int ch) { MChunk t{};
  for (int u = 0; u < 8; u++) for (int lane = 0; lane < 64; lane++) { const int dl = ch * 8 + u; const bool sp = dl == 0; const int n = lane & 15, g = lane >> 4; u16 f[4] = {}, iv[4] = {};
    for (int j = 0; j < 4; j++) { const int k = 4 * g + j; const bool ok = !sp || ((n < 8) == (k < 8));
      { const int a = (sp ? (k & 7) : (k >> 1)) * jp_(sp, dl, n); const bool ii = !sp && (k & 1), io = (n & 1) && (!sp || n >= 2);
        const double v = (ii == io) ? c2048(a) : (ii ? s2048(a) : -s2048(a)); f[j] = tobf(ok ? v : 0.0); }
      { const int a = (sp ? (n & 7) : (n >> 1)) * jp_(sp, dl, k); const bool ii = (k & 1) && (!sp || k >= 2), io = !sp && (n & 1);
        const double v = (ii == io) ? c2048(a) : (ii ? -s2048(a) : s2048(a)); iv[j] = tobf(ok ? v * ((sp && k < 2 ? 1.0 : 2.0) / 2097152.0) : 0.0); } }
    unsigned* o = &t.v[(u * 64 + lane) * 4]; o[0] = f[0] | ((unsigned)f[1] << 16); o[1] = f[2] | ((unsigned)f[3] << 16); o[2] = iv[0] | ((unsigned)iv[1] << 16); o[3] = iv[2] | ((unsigned)iv[3] << 16); }
  return t; }
template <int I> constexpr MChunk MCH = mkm(I);
struct MBig { MChunk c[16]; };
}
__device__ __attribute__((aligned(1024))) const ct::CTab CTAB = ct::mkctab();
__device__ __attribute__((aligned(1024))) const ct::MBig MTAB = {{ct::MCH<0>, ct::MCH<1>, ct::MCH<2>, ct::MCH<3>, ct::MCH<4>, ct::MCH<5>, ct::MCH<6>, ct::MCH<7>, ct::MCH<8>, ct::MCH<9>, ct::MCH<10>, ct::MCH<11>, ct::MCH<12>, ct::MCH<13>, ct::MCH<14>, ct::MCH<15>}};

DI float bf2f(u16 v) { return __uint_as_float(((unsigned)v) << 16); }
DI unsigned pk2(float lo, float hi) { f32x2 v = {lo, hi}; bf16x2 b = __builtin_convertvector(v, bf16x2); return __builtin_bit_cast(unsigned, b); }
DI float rbf(float f) { return __uint_as_float(pk2(f, 0.f) << 16); }
DI rsrc_t mkrs(const void* p, unsigned bytes) { return __builtin_amdgcn_make_buffer_rsrc((void*)p, 0, bytes, 0x00020000); }
DI void dma16(rsrc_t rs, LAS char* dst, unsigned voff, unsigned soff) {
  asm volatile("s_mov_b32 m0, %0\n\ts_nop 0\n\tbuffer_load_dwordx4 %1, %2, %3 offen lds" :: "s"((unsigned)(size_t)dst), "v"(voff), "s"(rs), "s"(soff) : "memory");
}
DI u32x4 gld16(const void* base, unsigned voff) { u32x4 r; asm volatile("global_load_dwordx4 %0, %1, %2" : "=v"(r) : "v"(voff), "s"(base) : "memory"); return r; }
DI unsigned gld2(const void* base, unsigned voff) { unsigned r; asm volatile("global_load_ushort %0, %1, %2" : "=v"(r) : "v"(voff), "s"(base) : "memory"); return r; }
DI s16x4 trd(LAS char* p) { return __builtin_amdgcn_ds_read_tr16_b64_v4i16((LAS s16x4*)p); }
DI bf16x8 cat8(s16x4 a, s16x4 b) { return __builtin_bit_cast(bf16x8, __builtin_shufflevector(a, b, 0, 1, 2, 3, 4, 5, 6, 7)); }
DI bf16x8 ld128(LAS char* p) { return *(LAS bf16x8*)p; }
#define MFMA16(a, b, c) __builtin_amdgcn_mfma_f32_16x16x32_bf16((a), (b), (c), 0, 0, 0)
#define MFMA32(a, b, c) __builtin_amdgcn_mfma_f32_32x32x16_bf16((a), (b), (c), 0, 0, 0)
#define WAITV(n) asm volatile("s_waitcnt vmcnt(" #n ")" ::: "memory")
#define WAITL(n) asm volatile("s_waitcnt lgkmcnt(" #n ")" ::: "memory")
#define WAITL0() __builtin_amdgcn_s_waitcnt(0xC07F)
#define BAR() __builtin_amdgcn_s_barrier()

DI unsigned pk2v(f32x2 v) { bf16x2 b = __builtin_convertvector(v, bf16x2); return __builtin_bit_cast(unsigned, b); }
DI unsigned gelu2(float a, float b) {
  const float c1 = -2.302208198f, c2 = -0.1029432397f;
  f32x2 v = {a, b};
  f32x2 t = v * v; f32x2 p = t * c2 + c1; f32x2 z = v * p;
  f32x2 e; e.x = __builtin_amdgcn_exp2f(z.x); e.y = __builtin_amdgcn_exp2f(z.y);
  f32x2 d = e + 1.f; f32x2 r; r.x = __builtin_amdgcn_rcpf(d.x); r.y = __builtin_amdgcn_rcpf(d.y);
  return pk2v(v * r);
}
DI float gelu_t(float v) {
  const float c1 = -2.302208198f, c2 = -0.1029432397f;
  float z = v * (c1 + c2 * v * v);
  float e = __builtin_amdgcn_exp2f(z);
  return v * __builtin_amdgcn_rcpf(1.f + e);
}

#define WST(i)
#define STAMP(k, i)

#define SB() __builtin_amdgcn_sched_barrier(0)
typedef __attribute__((address_space(1))) unsigned gu32;
constexpr int FLAG_BDONE = 0;
constexpr int FLAG_PAIR = 32;

constexpr int G1_XB = 16384, G1_WB = 32768, G1_W0 = 3 * G1_XB, G1_BI = G1_W0 + 3 * G1_WB, G1_CNT = G1_BI + 1024, G1_LDS = G1_CNT + 16;
__global__ __launch_bounds__(512, 2) void k_gemm1(const u16* __restrict__ x, const u16* __restrict__ w1, const u16* __restrict__ b1, const u16* __restrict__ gw,
                                                  u16* __restrict__ h, u16* __restrict__ T, float* __restrict__ g, unsigned* __restrict__ flags) {
  extern __shared__ __attribute__((aligned(16))) char smem[];
  LAS char* lds = (LAS char*)smem;
  const int tid = threadIdx.x, lane = tid & 63, wid = __builtin_amdgcn_readfirstlane(tid >> 6);
  const int b = blockIdx.x, xcd = b & 7, idx = b >> 3;
  const int mb = (xcd & 1) * 8 + (idx & 7), nb = (xcd >> 1) * 4 + (idx >> 3);
  STAMP(1, 0);
  if (wid >= 4) {
    const int lw = wid - 4;
    {
      const int beta = mb >> 1, alpha0 = (mb & 1) * 128;
      const unsigned xo = (8 * alpha0 + beta) * D_;
      const unsigned wo = (nb * 448) & ~127u;
      rsrc_t rx = mkrs(x + xo, (S_ * D_ - xo) * 2);
      rsrc_t rw = mkrs((const char*)w1 + wo, D_ * F_ * 2 - wo);
      rsrc_t rb = mkrs(b1 + nb * 224, 448);
      unsigned vx[2], vw[8];
      { const int r = lane >> 3, c1 = lane & 7;
        vx[0] = r * 16384 + ((c1 ^ (r >> 1)) * 16); vx[1] = r * 16384 + ((c1 ^ (4 + (r >> 1))) * 16); }
#pragma unroll
      for (int i = 0; i < 8; i++) { const int hi = lane >> 5, k = 2 * i + hi, f = (k & 3) | (((k >> 3) & 1) << 2);
        vw[i] = hi * (F_ * 2) + (((lane & 31) ^ (f << 1)) * 16); }
      auto stage = [&](int kt, int buf) {
#pragma unroll
        for (int i = 0; i < 4; i++) { const int j = lw * 4 + i; dma16(rx, lds + buf * G1_XB + j * 1024, vx[i & 1], j * 8 * 16384 + kt * 128); }
#pragma unroll
        for (int i = 0; i < 8; i++) { const int j = lw * 8 + i; dma16(rw, lds + G1_W0 + buf * G1_WB + j * 1024, vw[i], (kt * 64 + 2 * j) * (F_ * 2)); }
      };
      if (lw == 0) dma16(rb, lds + G1_BI, lane * 16, 0);
      stage(0, 0); stage(1, 1); stage(2, 2);
      if (lw == 1) {
        if (lane == 0) *(LAS unsigned*)(lds + G1_CNT) = 0u;
        if (b < 129 && lane == 0) flags[b * 32] = 0u;
      }
      WAITV(24); WAITL(0); BAR();
      int cb = 0;
      for (int kt = 0; kt < 15; kt++) {
        if (kt < 14) WAITV(12); else WAITV(0);
        BAR();
        if (kt + 3 < 16) stage(kt + 3, cb);
        cb = (cb == 2) ? 0 : cb + 1;
      }
    }
    const int beta = b & 7, dblk = (b >> 3) & 15, dh = b >> 7;
    LAS char* tl = lds + G1_W0 + G1_WB;
    {
      rsrc_t rx = mkrs(x + beta * D_ + dblk * 64, (unsigned)(S_ * D_ * 2 - (beta * D_ + dblk * 64) * 2));
      const int hi = lane >> 5, r = (lane & 31) >> 2, c = (lane & 3) ^ (2 * ((r >> 2) & 1));
      const unsigned voff = r * 16384 + hi * 64 + c * 16;
#pragma unroll
      for (int i = 0; i < 8; i++) { const int j = lw * 8 + i; dma16(rx, tl + j * 1024, voff, j * 8 * 16384); }
    }
    bf16x8 ef[2][8];
    {
      const u32x4* et = (const u32x4*)&ETAB1;
#pragma unroll
      for (int c = 0; c < 2; c++)
#pragma unroll
        for (int kb = 0; kb < 8; kb++) ef[c][kb] = __builtin_bit_cast(bf16x8, et[((dh * 8 + lw * 2 + c) * 8 + kb) * 64 + lane]);
    }
    {
      const u32x4* gp = (const u32x4*)gw;
      u32x4 g0 = gp[lane * 2], g1 = gp[lane * 2 + 1];
#pragma unroll
      for (int rr = 0; rr < 2; rr++) {
        const int s = b * 8 + lw * 2 + rr;
        const u32x4* xp = (const u32x4*)(x + s * D_);
        u32x4 x0 = xp[lane * 2], x1 = xp[lane * 2 + 1];
        float acc = 0.f;
#pragma unroll
        for (int e = 0; e < 4; e++) {
          acc += rbf(__uint_as_float(x0[e] << 16) * __uint_as_float(g0[e] << 16));
          acc += rbf(__uint_as_float(x0[e] & 0xffff0000u) * __uint_as_float(g0[e] & 0xffff0000u));
          acc += rbf(__uint_as_float(x1[e] << 16) * __uint_as_float(g1[e] << 16));
          acc += rbf(__uint_as_float(x1[e] & 0xffff0000u) * __uint_as_float(g1[e] & 0xffff0000u));
        }
#pragma unroll
        for (int o = 32; o > 0; o >>= 1) acc += __shfl_xor(acc, o);
        if (lane == 0) { float z = rbf(acc); g[256 * (s & 7) + (s >> 3)] = rbf(1.f / (1.f + __expf(-z))); }
      }
    }
    WAITV(0);
    if (lane == 0) __hip_atomic_fetch_add((LAS unsigned*)(lds + G1_CNT), 1u, __ATOMIC_RELAXED, __HIP_MEMORY_SCOPE_WORKGROUP);
    while (__builtin_amdgcn_readfirstlane(*(volatile LAS unsigned*)(lds + G1_CNT)) < 4u) __builtin_amdgcn_s_sleep(1);
    asm volatile("" ::: "memory");
    const int gq = lane >> 4, q = (lane & 15) >> 2, p = lane & 3;
    f32x4 acc[2][4];
#pragma unroll
    for (int c = 0; c < 2; c++)
#pragma unroll
      for (int i = 0; i < 4; i++) acc[c][i] = (f32x4){0.f, 0.f, 0.f, 0.f};
    LAS char* base = tl + (gq >> 1) * 2 * 512 + (4 * (gq & 1) + q) * 64 + ((p ^ (2 * (gq & 1))) * 16);
#pragma unroll
    for (int kb = 0; kb < 8; kb++) {
#pragma unroll
      for (int i = 0; i < 4; i++) {
        LAS char* a = base + (4 * kb) * 1024 + (i >> 1) * 512 + (i & 1) * 8;
        bf16x8 A = cat8(trd(a), trd(a + 2 * 1024));
#pragma unroll
        for (int c = 0; c < 2; c++) acc[c][i] = MFMA16(A, ef[c][kb], acc[c][i]);
      }
    }
#pragma unroll
    for (int c = 0; c < 2; c++) {
      const int dp = dh * 128 + lw * 32 + c * 16 + (lane & 15);
#pragma unroll
      for (int ip = 0; ip < 2; ip++) {
        u32x4 w;
        w[0] = pk2(acc[c][2 * ip][0], acc[c][2 * ip][1]); w[1] = pk2(acc[c][2 * ip][2], acc[c][2 * ip][3]);
        w[2] = pk2(acc[c][2 * ip + 1][0], acc[c][2 * ip + 1][1]); w[3] = pk2(acc[c][2 * ip + 1][2], acc[c][2 * ip + 1][3]);
        *(u32x4*)(T + (beta * 256 + dp) * D_ + dblk * 64 + ip * 32 + gq * 8) = w;
      }
    }
    return;
  }
  const int wm = wid >> 1, wn = wid & 1;
  const int c = lane & 15, gq = lane >> 4, q = (lane & 15) >> 2, p = lane & 3;
  LAS char* xa0 = lds + (64 * wm + c) * 128 + ((gq ^ (c >> 1)) * 16);
  LAS char* xa1 = lds + (((64 * wm + c) * 128 + ((gq ^ (c >> 1)) * 16)) ^ 64);
  const unsigned wa0 = G1_W0 + (8 * gq + q) * 512 + ((q | ((gq & 1) << 2)) << 5) + ((p >> 1) << 4) + ((p & 1) << 3);
  const unsigned tsh = (7 * wn + 2 * (nb & 1));
  unsigned wa[7];
#pragma unroll
  for (int t = 0; t < 7; t++) wa[t] = wa0 ^ ((tsh + t) << 5);
  f32x4 acc[7][4];
  bf16x8 xf[2][4];
  s16x4 wl[2][7], wh[2][7];
  BAR();
  STAMP(1, 1);
#pragma unroll
  for (int t = 0; t < 7; t++) { u32x2 bv = *(LAS u32x2*)(lds + G1_BI + (16 * (7 * wn + t) + 4 * gq) * 2);
    f32x4 bb = {__uint_as_float(bv[0] << 16), __uint_as_float(bv[0] & 0xffff0000u), __uint_as_float(bv[1] << 16), __uint_as_float(bv[1] & 0xffff0000u)};
#pragma unroll
    for (int i = 0; i < 4; i++) acc[t][i] = bb; }
#define RDX(set, buf, ks, i) xf[set][i] = ld128(((ks) ? xa1 : xa0) + (buf) * G1_XB + (i) * 2048)
#define RDWL(set, buf, ks, t) wl[set][t] = trd(lds + wa[t] + ((buf) & 1) * G1_WB + (ks) * 16384)
#define RDWH(set, buf, ks, t) wh[set][t] = trd(lds + wa[t] + ((buf) & 1) * G1_WB + (ks) * 16384 + 2048)
#define MM(set, t, i) acc[t][i] = MFMA16(cat8(wl[set][t], wh[set][t]), xf[set][i], acc[t][i])
#define RDS(ns, buf, ks, n) do { if ((n) < 4) RDX(ns, buf, ks, (n) & 3); else if ((n) < 18) { if (((n) & 1) == 0) RDWL(ns, buf, ks, (((n) - 4) >> 1) % 7); else RDWH(ns, buf, ks, (((n) - 4) >> 1) % 7); } } while (0)
#define PHASE(cs, ns, buf, ks, DO_RD, ADJ) do { \
    _Pragma("unroll") for (int s_ = 0; s_ < 28; s_++) { MM(cs, s_ >> 2, s_ & 3); if (DO_RD) RDS(ns, buf, ks, s_); \
      if ((ADJ) != 0 && s_ >= 18 && s_ < 25) { wa[(s_ - 18) % 7] += (ADJ); asm volatile("" : "+v"(wa[(s_ - 18) % 7])); } SB(); } \
    WAITL0(); } while (0)
#pragma unroll
  for (int n = 0; n < 18; n++) RDS(0, 0, 0, n);
  WAITL0();
  for (int k3 = 0; k3 < 5; k3++) {
    PHASE(0, 1, 0, 1, true, 0); BAR(); SB(); PHASE(1, 0, 1, 0, true, 0);
    PHASE(0, 1, 1, 1, true, 2 * G1_WB); BAR(); SB(); PHASE(1, 0, 2, 0, true, 0);
    PHASE(0, 1, 2, 1, true, -2 * G1_WB); BAR(); SB(); PHASE(1, 0, 0, 0, true, 0);
  }
  PHASE(0, 1, 0, 1, true, 0);
  PHASE(1, 0, 0, 0, false, 0);
  STAMP(1, 2);
#pragma unroll
  for (int i = 0; i < 4; i++) {
    char* hb = (char*)h + ((size_t)(8 * mb + 4 * wm + i) * 112 + 7 * nb) * 1024 + lane * 16;
    unsigned pk[7][2];
#pragma unroll
    for (int t = 0; t < 7; t++) { pk[t][0] = gelu2(acc[t][i][0], acc[t][i][1]); pk[t][1] = gelu2(acc[t][i][2], acc[t][i][3]); }
    if (wn == 0) {
#pragma unroll
      for (int pr = 0; pr < 3; pr++) *(u32x4*)(hb + pr * 1024) = (u32x4){pk[2 * pr][0], pk[2 * pr][1], pk[2 * pr + 1][0], pk[2 * pr + 1][1]};
      *(u32x2*)(hb + 3 * 1024) = (u32x2){pk[6][0], pk[6][1]};
    } else {
      *(u32x2*)(hb + 3 * 1024 + 8) = (u32x2){pk[0][0], pk[0][1]};
#pragma unroll
      for (int pr = 0; pr < 3; pr++) *(u32x4*)(hb + (4 + pr) * 1024) = (u32x4){pk[2 * pr + 1][0], pk[2 * pr + 1][1], pk[2 * pr + 2][0], pk[2 * pr + 2][1]};
    }
  }
  STAMP(1, 3);
}

DI int crow(int r, int h) { return (r & 3) + 8 * (r >> 2) + 4 * h; }
DI bf16x8 pack8(const f32x16& v, int s) { u32x4 w; w[0] = pk2(v[8 * s], v[8 * s + 1]); w[1] = pk2(v[8 * s + 2], v[8 * s + 3]); w[2] = pk2(v[8 * s + 4], v[8 * s + 5]); w[3] = pk2(v[8 * s + 6], v[8 * s + 7]); return __builtin_bit_cast(bf16x8, w); }
DI unsigned ploff(int pl, unsigned bb) { return pl * 2048 + (bb ^ ((pl & 7) << 5)); }
DI unsigned ploff2(int pl, unsigned bb) { return pl * 2048 + (bb ^ (pl << 3)); }
DI unsigned ploff3(int pl, unsigned bb) { return pl * 2048 + (bb ^ (((bb >> 7) & 7) << 3) ^ ((pl & 1) << 3) ^ (((pl >> 1) & 3) << 6)); }
typedef unsigned u32x4u __attribute__((ext_vector_type(4), aligned(2)));
DI unsigned swp16(unsigned v) { return __builtin_amdgcn_alignbit(v, v, 16); }
DI float bfe(s16x4 v, int e) { return __uint_as_float(((unsigned)(u16)v[e]) << 16); }
struct cplx16 { f32x16 re, im; };
struct frag2 { bf16x8 r[2], i[2]; };
DI frag2 packc(const cplx16& c) { frag2 f; f.r[0] = pack8(c.re, 0); f.r[1] = pack8(c.re, 1); f.i[0] = pack8(c.im, 0); f.i[1] = pack8(c.im, 1); return f; }
template <class F>
DI void spectral_unit(LAS char* pls, LAS char* fs_own, LAS char* cst, const int dl, const int wid, const int lane, const u16* __restrict__ T, const u16* __restrict__ wr, const u16* __restrict__ wi, u16* __restrict__ T2, F&& dmaq) {
  STAMP(2, 0); WST(0);
  const bool sp = (dl == 0);
  const bool packed = sp && wid == 0;
  const int l31 = lane & 31, hh = lane >> 5;
  struct raw_f { u32x4 v[4]; unsigned x[4]; };
  struct filt { s16x4 re[4], im[4]; float x[4]; };
  auto filter_issue = [&](int j) { raw_f w; const int jm = (2048 - j) & 2047;
    const unsigned od = (j * DF_ + 8 * lane) * 2, om = (jm * DF_ + 8 * lane + 1) * 2;
    w.v[0] = gld16(wr, od); w.v[1] = gld16(wi, od); w.v[2] = gld16(wr, om); w.v[3] = gld16(wi, om);
    w.x[0] = gld2(wr, jm * DF_ * 2); w.x[1] = gld2(wi, jm * DF_ * 2); w.x[2] = gld2(wr, (j * DF_ + 512) * 2); w.x[3] = gld2(wi, (j * DF_ + 512) * 2);
    return w; };
  auto filter_stage = [&](LAS char* fs, const raw_f& w) { filt f;
    *(LAS u32x4*)(fs + lane * 16) = w.v[0];
    *(LAS u32x4*)(fs + 2048 + lane * 16) = w.v[1];
    *(LAS u32x4*)(fs + 2032 - lane * 16) = (u32x4){swp16(w.v[2][3]), swp16(w.v[2][2]), swp16(w.v[2][1]), swp16(w.v[2][0])};
    *(LAS u32x4*)(fs + 2048 + 2032 - lane * 16) = (u32x4){swp16(w.v[3][3]), swp16(w.v[3][2]), swp16(w.v[3][1]), swp16(w.v[3][0])};
    asm volatile("" ::: "memory");
    const int blk = (lane >> 4) & 1, q = (lane & 15) >> 2, p = lane & 3;
#pragma unroll
    for (int G = 0; G < 4; G++) { const unsigned off = (8 * G + 4 * hh + q) * 64 + (16 * blk + 4 * p) * 2; f.re[G] = trd(fs + off); f.im[G] = trd(fs + 2048 + off); }
#pragma unroll
    for (int i = 0; i < 4; i++) f.x[i] = __uint_as_float(w.x[i] << 16);
    return f; };
  {
    rsrc_t rt = mkrs(T, 8 * 256 * 1024 * 2);
#pragma unroll
    for (int i = 0; i < 4; i++) { const int j = wid * 4 + i, pl = j >> 1, hf = j & 1;
      const int row = sp ? ((pl & 7) * 256 + (pl >> 3)) : ((pl >> 1) * 256 + 2 * dl + (pl & 1));
      dma16(rt, pls + pl * 2048 + hf * 1024, (lane ^ ((pl & 7) << 1)) * 16, row * 2048 + hf * 1024); }
  }
  const int jrow = sp ? (wid < 4 ? 256 * wid : 128 + 256 * (wid - 4)) : dl + 256 * wid;
  raw_f rwA = filter_issue(jrow), rwC;
  if (packed) rwC = filter_issue(1024);
  {
    rsrc_t rc = mkrs(&CTAB, 12 * 1024), rm = mkrs((const char*)&MTAB + dl * 1024, 1024);
    dma16(rc, cst + wid * 1024, lane * 16, wid * 1024);
    if (wid < 4) dma16(rc, cst + (8 + wid) * 1024, lane * 16, (8 + wid) * 1024);
    else dma16(rm, cst + 12 * 1024, lane * 16, 0);
  }
  dmaq(0); dmaq(1);
  WST(1);
  asm volatile("s_waitcnt vmcnt(8)" : "+v"(rwA.v[0]), "+v"(rwA.v[1]), "+v"(rwA.v[2]), "+v"(rwA.v[3]), "+v"(rwA.x[0]), "+v"(rwA.x[1]), "+v"(rwA.x[2]), "+v"(rwA.x[3]) :: "memory");
  if (packed) asm volatile("" : "+v"(rwC.v[0]), "+v"(rwC.v[1]), "+v"(rwC.v[2]), "+v"(rwC.v[3]), "+v"(rwC.x[0]), "+v"(rwC.x[1]), "+v"(rwC.x[2]), "+v"(rwC.x[3]) :: "memory");
  WST(3);
  filt fA = filter_stage(fs_own, rwA);
  WAITL0();
  WST(4);
  BAR();
  WST(5);
  STAMP(2, 1);
  u32x4 ct_[12], mt_;
  mt_ = *(LAS u32x4*)(cst + 12 * 1024 + lane * 16);
#pragma unroll
  for (int i = 0; i < 12; i++) ct_[i] = *(LAS u32x4*)(cst + i * 1024 + lane * 16);
  const s16x4 Mf = __builtin_bit_cast(s16x4, (u32x2){mt_[0], mt_[1]}), Mi = __builtin_bit_cast(s16x4, (u32x2){mt_[2], mt_[3]});
  bf16x8 Cc[2], Sn[2], nS[2];
#pragma unroll
  for (int s = 0; s < 2; s++) { Cc[s] = __builtin_bit_cast(bf16x8, ct_[s]); Sn[s] = __builtin_bit_cast(bf16x8, ct_[2 + s]); nS[s] = __builtin_bit_cast(bf16x8, ct_[2 + s] ^ 0x80008000u); }
  f32x2 twc[8], tws[8];
#pragma unroll
  for (int r = 0; r < 8; r++) { twc[r] = (f32x2){__uint_as_float(ct_[4 + (r >> 1)][(2 * r) & 3]), __uint_as_float(ct_[4 + (r >> 1)][(2 * r + 1) & 3])};
                                tws[r] = (f32x2){__uint_as_float(ct_[8 + (r >> 1)][(2 * r) & 3]), __uint_as_float(ct_[8 + (r >> 1)][(2 * r + 1) & 3])}; }
  { int ln = lane; asm volatile("" : "+v"(ln)); const int g = ln >> 4, q = (ln & 15) >> 2, p = ln & 3, n = ln & 15;
    s16x4 a[8];
#pragma unroll
    for (int t = 0; t < 8; t++) a[t] = trd(pls + ploff(4 * g + q, (wid * 128 + t * 16 + 4 * p) * 2));
#pragma unroll
    for (int t = 0; t < 8; t++) {
      f32x4 dd = __builtin_amdgcn_mfma_f32_16x16x16bf16_1k(a[t], Mf, (f32x4){0.f, 0.f, 0.f, 0.f}, 0, 0, 0);
      *(LAS u32x2*)(pls + ploff2(n, (wid * 128 + t * 16 + 4 * g) * 2)) = (u32x2){pk2(dd[0], dd[1]), pk2(dd[2], dd[3])}; } }
  WAITL0(); WST(6); BAR(); WST(7);
  STAMP(2, 2);
  {
    const f32x16 z = {0.f};
    int ln0 = lane; asm volatile("" : "+v"(ln0));
    const int blk = (ln0 >> 4) & 1, q = (ln0 & 15) >> 2, p = ln0 & 3;
    const int pA = 2 * wid;
    frag2 X;
    { const int hh = ln0 >> 5;
#pragma unroll
      for (int s = 0; s < 2; s++) { const unsigned off = (16 * s + 4 * hh + q) * 64 + (16 * blk + 4 * p) * 2;
        X.r[s] = cat8(trd(pls + ploff2(pA, off)), trd(pls + ploff2(pA, off + 512)));
        X.i[s] = cat8(trd(pls + ploff2(pA + 1, off)), trd(pls + ploff2(pA + 1, off + 512))); } }
    auto stA = [&](const frag2& X, bool inv) { cplx16 c; const bf16x8* P = inv ? nS : Sn; const bf16x8* N = inv ? Sn : nS;
      c.re = MFMA32(X.r[0], Cc[0], z); c.im = MFMA32(X.i[0], Cc[0], z); c.re = MFMA32(X.r[1], Cc[1], c.re); c.im = MFMA32(X.i[1], Cc[1], c.im);
      c.re = MFMA32(X.i[0], P[0], c.re); c.im = MFMA32(X.r[0], N[0], c.im); c.re = MFMA32(X.i[1], P[1], c.re); c.im = MFMA32(X.r[1], N[1], c.im); return c; };
    auto stB = [&](const frag2& X) { cplx16 c;
      c.re = MFMA32(Cc[0], X.r[0], z); c.im = MFMA32(Cc[0], X.i[0], z); c.re = MFMA32(Cc[1], X.r[1], c.re); c.im = MFMA32(Cc[1], X.i[1], c.im);
      c.re = MFMA32(Sn[0], X.i[0], c.re); c.im = MFMA32(nS[0], X.r[0], c.im); c.re = MFMA32(Sn[1], X.i[1], c.re); c.im = MFMA32(nS[1], X.r[1], c.im); return c; };
    auto cmul_pack = [&](const cplx16& c, auto&& coef) { frag2 f; u32x4 wr_[2], wi_[2];
#pragma unroll
      for (int r = 0; r < 8; r++) { f32x2 cr, ci; coef(r, cr, ci);
        const f32x2 a = {c.re[2 * r], c.re[2 * r + 1]}, b = {c.im[2 * r], c.im[2 * r + 1]};
        wr_[r >> 2][r & 3] = pk2v(a * cr - b * ci); wi_[r >> 2][r & 3] = pk2v(a * ci + b * cr); }
      f.r[0] = __builtin_bit_cast(bf16x8, wr_[0]); f.r[1] = __builtin_bit_cast(bf16x8, wr_[1]); f.i[0] = __builtin_bit_cast(bf16x8, wi_[0]); f.i[1] = __builtin_bit_cast(bf16x8, wi_[1]);
      return f; };
    auto fval = [&](const filt& f, int r, float& fr, float& fi) {
      fr = bfe(f.re[r >> 2], r & 3); fi = bfe(f.im[r >> 2], r & 3); if (r >= 8) fi = -fi;
      if (r == 0) { const float ar = 0.5f * (fr + f.x[0]), ai = 0.5f * (fi - f.x[1]); fr = lane == 0 ? ar : fr; fi = lane == 0 ? ai : fi; }
      if (r == 8) { const float ar = 0.5f * (fr + f.x[2]), ai = 0.5f * (fi + f.x[3]); fr = lane == 0 ? ar : fr; fi = lane == 0 ? ai : fi; } };
    cplx16 A = stA(X, false);
    dmaq(2);
    X = cmul_pack(A, [&](int r, f32x2& cr, f32x2& ci) { cr = twc[r]; ci = -tws[r]; });
    A = stB(X);
    dmaq(3);
    if (packed) {
      LAS char* sc = pls;
      int ln = lane; asm volatile("" : "+v"(ln)); const int l31 = ln & 31, hh = ln >> 5;
      WAITL0();
      filt fC = filter_stage(sc, rwC);
      WAITL0(); asm volatile("" ::: "memory");
      f32x16 zr;
#pragma unroll
      for (int r = 0; r < 16; r++) *(LAS float*)(sc + (32 * crow(r, hh) + l31) * 4) = A.re[r];
      WAITL0(); asm volatile("" ::: "memory");
#pragma unroll
      for (int r = 0; r < 16; r++) zr[r] = *(LAS float*)(sc + ((1024 - (32 * crow(r, hh) + l31)) & 1023) * 4);
      WAITL0(); asm volatile("" : "+v"(zr) :: "memory");
#pragma unroll
      for (int r = 0; r < 16; r++) *(LAS float*)(sc + (32 * crow(r, hh) + l31) * 4) = A.im[r];
      WAITL0(); asm volatile("" ::: "memory");
#pragma unroll
      for (int r = 0; r < 16; r++) { float ar, ai, br, bi; fval(fA, r, ar, ai); fval(fC, r, br, bi);
        const float zi = -*(LAS float*)(sc + ((1024 - (32 * crow(r, hh) + l31)) & 1023) * 4);
        const float alr = 0.5f * (ar + br), ali = 0.5f * (ai + bi), ber = 0.5f * (ar - br), bei = 0.5f * (ai - bi);
        const float a = A.re[r], b = A.im[r];
        A.re[r] = a * alr - b * ali + zr[r] * ber - zi * bei; A.im[r] = a * ali + b * alr + zr[r] * bei + zi * ber;
        if (r & 1) { asm volatile("" : "+v"(A.re[r]), "+v"(A.im[r]), "+v"(A.re[r - 1]), "+v"(A.im[r - 1]) :: "memory"); SB(); } }
      WAITL0(); asm volatile("" ::: "memory");
      X = packc(A);
    } else {
      X = cmul_pack(A, [&](int r, f32x2& cr, f32x2& ci) { float a0, b0, a1, b1; fval(fA, 2 * r, a0, b0); fval(fA, 2 * r + 1, a1, b1); cr = (f32x2){a0, a1}; ci = (f32x2){b0, b1}; });
    }
    A = stA(X, true);
    X = cmul_pack(A, [&](int r, f32x2& cr, f32x2& ci) { cr = twc[r]; ci = tws[r]; });
    A = stA(X, true);
    { int ln = lane; asm volatile("" : "+v"(ln)); const int l31 = ln & 31, hh = ln >> 5;
#pragma unroll
      for (int G = 0; G < 4; G++) { const unsigned off = (32 * l31 + 8 * G + 4 * hh) * 2;
        *(LAS u32x2*)(pls + ploff3(pA, off)) = (u32x2){pk2(A.re[4 * G], A.re[4 * G + 1]), pk2(A.re[4 * G + 2], A.re[4 * G + 3])};
        *(LAS u32x2*)(pls + ploff3(pA + 1, off)) = (u32x2){pk2(A.im[4 * G], A.im[4 * G + 1]), pk2(A.im[4 * G + 2], A.im[4 * G + 3])}; } }
  }
  WAITL0(); WST(8); BAR(); WST(9);
  STAMP(2, 3);
  { int ln = lane; asm volatile("" : "+v"(ln)); const int g = ln >> 4, q = (ln & 15) >> 2, p = ln & 3, n = ln & 15;
    const int row = sp ? ((n & 7) * 256 + (n >> 3)) : ((n >> 1) * 256 + 2 * dl + (n & 1));
    rsrc_t ro = mkrs(T2, 8 * 256 * 1024 * 2);
    s16x4 a0[4], a1[4];
#pragma unroll
    for (int t = 0; t < 4; t++) { const int d0 = wid * 128 + t * 32; a0[t] = trd(pls + ploff3(4 * g + q, (d0 + 8 * p) * 2)); a1[t] = trd(pls + ploff3(4 * g + q, (d0 + 8 * p + 4) * 2)); }
#pragma unroll
    for (int t = 0; t < 4; t++) { const int d0 = wid * 128 + t * 32;
      f32x4 d0v = __builtin_amdgcn_mfma_f32_16x16x16bf16_1k(a0[t], Mi, (f32x4){0.f, 0.f, 0.f, 0.f}, 0, 0, 0);
      f32x4 d1v = __builtin_amdgcn_mfma_f32_16x16x16bf16_1k(a1[t], Mi, (f32x4){0.f, 0.f, 0.f, 0.f}, 0, 0, 0);
      __builtin_amdgcn_raw_buffer_store_b128((u32x4){pk2(d0v[0], d0v[1]), pk2(d0v[2], d0v[3]), pk2(d1v[0], d1v[1]), pk2(d1v[2], d1v[3])}, ro, (row * 1024 + d0 + 8 * g) * 2, 0, 16); } }
  WST(10);
  STAMP(2, 4);
}

constexpr int G2_B = 16384, G2_W0 = 4 * G2_B, G2_PL = 8 * G2_B, G2_LDS = G2_PL + 32768;
#define MAP_MIXED 1
#define K0STEPS 18
constexpr int NP = K0STEPS, NM = 56 - K0STEPS, NF = NM + 4;
__global__ __launch_bounds__(512, 2) void k_gemm2(const u16* __restrict__ h, const u16* __restrict__ w2, const u16* __restrict__ b2, const u16* __restrict__ T,
                                                  const u16* __restrict__ wr, const u16* __restrict__ wi, u16* __restrict__ T2,
                                                  const float* __restrict__ g, u16* __restrict__ out, float* __restrict__ part, unsigned* __restrict__ flags) {
  extern __shared__ __attribute__((aligned(16))) char smem[];
  LAS char* lds = (LAS char*)smem;
  const int tid = threadIdx.x, lane = tid & 63, wid = __builtin_amdgcn_readfirstlane(tid >> 6);
  const int b = blockIdx.x, xcd = b & 7, idx = b >> 3;
  const int kh = idx & 1, mb = (xcd & 3) * 4 + ((idx >> 1) & 3), nb = (xcd >> 2) * 4 + (idx >> 3);
  const int pair = mb * 8 + nb;
  const int beta = mb >> 1, alpha0 = (mb & 1) * 128, d0 = nb * 128;
  const bool isP = (kh == 0);
  STAMP(3, 0);
  rsrc_t rh = mkrs((const char*)h + (size_t)mb * 8 * 112 * 1024, 8 * 112 * 1024);
  rsrc_t rw = mkrs(w2 + d0, (F_ * D_ - d0) * 2);
  const unsigned vh = lane * 16;
  unsigned vw[2];
#pragma unroll
  for (int i = 0; i < 2; i++) { const int r = lane >> 4, k = 4 * i + r; vw[i] = r * 2048 + (((lane & 15) ^ ((k & 7) << 1)) * 16); }
  if (isP) {
    spectral_unit(lds + G2_PL, lds + (wid < 4 ? 3 * G2_B + wid * 4096 : G2_W0 + 3 * G2_B + (wid - 4) * 4096), lds + 2 * G2_B, pair, wid, lane, T, wr, wi, T2, [&](int st) {
#pragma unroll
      for (int i = 0; i < 2; i++) { const int j = wid * 2 + i; dma16(rh, lds + st * G2_B + j * 1024, vh, ((j >> 1) * 112 + 2 * st + (j & 1)) * 1024); }
#pragma unroll
      for (int i = 0; i < 2; i++) { const int j = wid * 2 + i; dma16(rw, lds + G2_W0 + st * G2_B + j * 1024, vw[i & 1], (st * 64 + j * 4) * 2048); }
    });
  }
  if (wid >= 4) {
    const int lw = wid - 4;
    auto stage = [&](int kk, int buf) {
#pragma unroll
      for (int i = 0; i < 4; i++) { const int j = lw * 4 + i; dma16(rh, lds + buf * G2_B + j * 1024, vh, ((j >> 1) * 112 + 2 * kk + (j & 1)) * 1024); }
#pragma unroll
      for (int i = 0; i < 4; i++) { const int j = lw * 4 + i; dma16(rw, lds + G2_W0 + buf * G2_B + j * 1024, vw[i & 1], (kk * 64 + j * 4) * 2048); }
    };
    if (isP) {
      WAITV(16); BAR();
      for (int kt = 0; kt < NP - 1; kt++) {
        if (kt == 0) WAITV(12); else if (kt < NP - 3) WAITV(16); else if (kt == NP - 3) WAITV(8); else WAITV(0);
        BAR();
        if (kt + 4 < NP) stage(kt + 4, kt & 3);
      }
      BAR();
      return;
    }
    rsrc_t re = mkrs((const char*)&ETAB2 + (mb & 1) * 8 * 8 * 1024, 8 * 8 * 1024);
    rsrc_t rt = mkrs(T2 + beta * 256 * 1024 + d0, (256 * 1024 - d0) * 2);
    rsrc_t rp = mkrs((const char*)part + (size_t)pair * 65536, 65536);
    stage(NP + 0, 0); stage(NP + 1, 1); stage(NP + 2, 2); stage(NP + 3, 3);
    WAITV(24); BAR();
    for (int kt = 0; kt < NF - 1; kt++) {
      if (kt < NF - 3 || kt >= NM) WAITV(16); else WAITV(16);
      if (kt == NF - 2) WAITV(16);
      BAR();
      const int s = kt + 4, buf = kt & 3;
      if (s < NM) stage(NP + s, buf);
      else if (s < NF) { const int k2 = s - NM;
#pragma unroll
        for (int i = 0; i < 4; i++) { const int j = lw * 4 + i; dma16(re, lds + buf * G2_B + j * 1024, vh, ((j >> 1) * 8 + 2 * k2 + (j & 1)) * 1024); }
#pragma unroll
        for (int i = 0; i < 4; i++) { const int j = lw * 4 + i; dma16(rt, lds + G2_W0 + buf * G2_B + j * 1024, vw[i & 1], (k2 * 64 + j * 4) * 2048); }
      } else if (s < NF + 2) { const int hf = s - NF;
#pragma unroll
        for (int i = 0; i < 4; i++) { const int j = lw * 4 + i; dma16(rp, lds + buf * G2_B + j * 1024, vh, (hf * 32 + j) * 1024); }
#pragma unroll
        for (int i = 0; i < 4; i++) { const int j = lw * 4 + i; dma16(rp, lds + G2_W0 + buf * G2_B + j * 1024, vh, (hf * 32 + 16 + j) * 1024); }
      }
    }
    WAITV(0); BAR();
    return;
  }
  const int wm = wid >> 1, wn = wid & 1;
  const int gq = lane >> 4, q = (lane & 15) >> 2, p = lane & 3;
  STAMP(3, 1);
  LAS char* ha = lds + (4 * wm) * 2048 + lane * 16;
  unsigned wa[4];
  { const int kl = 4 * gq + q;
    const unsigned a0 = G2_W0 + kl * 256 + ((kl & 7) << 5) + ((p >> 1) << 4) + ((p & 1) << 3);
#pragma unroll
    for (int t = 0; t < 4; t++) wa[t] = a0 ^ ((4 * wn + t) << 5); }
  f32x4 acc[4][4];
#pragma unroll
  for (int t = 0; t < 4; t++)
#pragma unroll
    for (int i = 0; i < 4; i++) acc[t][i] = (f32x4){0.f, 0.f, 0.f, 0.f};
  bf16x8 hf[2][4];
  s16x4 wl[2][4], wh[2][4];
#define G2RDH(set, buf, ks, i) hf[set][i] = ld128(ha + (buf) * G2_B + (i) * 2048 + (ks) * 1024)
#define G2RDWL(set, buf, ks, t) wl[set][t] = trd(lds + wa[t] + (buf) * G2_B + (ks) * 8192)
#define G2RDWH(set, buf, ks, t) wh[set][t] = trd(lds + wa[t] + (buf) * G2_B + (ks) * 8192 + 4096)
#define G2RDS(ns, buf, ks, n) do { if ((n) < 4) G2RDH(ns, buf, ks, (n) & 3); else if ((n) < 12) { if (((n) & 1) == 0) G2RDWL(ns, buf, ks, (((n) - 4) >> 1) & 3); else G2RDWH(ns, buf, ks, (((n) - 4) >> 1) & 3); } } while (0)
#define G2PHASE(A, cs, ns, buf, ks, DO_RD) do { \
    _Pragma("unroll") for (int s_ = 0; s_ < 16; s_++) { A[s_ >> 2][s_ & 3] = MFMA16(cat8(wl[cs][s_ >> 2], wh[cs][s_ >> 2]), hf[cs][s_ & 3], A[s_ >> 2][s_ & 3]); if (DO_RD) G2RDS(ns, buf, ks, s_); SB(); } \
    WAITL0(); } while (0)
#define G2STEP(A, buf) do { G2PHASE(A, 0, 1, buf, 1, true); BAR(); SB(); G2PHASE(A, 1, 0, ((buf) + 1) & 3, 0, true); } while (0)
#define G2LAST(A, buf) do { G2PHASE(A, 0, 1, buf, 1, true); G2PHASE(A, 1, 0, 0, 0, false); } while (0)
  if (isP) {
    WAITV(16); BAR();
#pragma unroll
    for (int n = 0; n < 12; n++) G2RDS(0, 0, 0, n);
    WAITL0();
    G2PHASE(acc, 0, 1, 0, 1, true); WAITV(12); BAR(); SB(); G2PHASE(acc, 1, 0, 1, 0, true);
    G2PHASE(acc, 0, 1, 1, 1, true); WAITV(8); BAR(); SB(); G2PHASE(acc, 1, 0, 2, 0, true);
    WAITV(0);
    G2PHASE(acc, 0, 1, 2, 1, true); BAR(); SB();
    if (tid == 0) __hip_atomic_fetch_add((gu32*)(flags + FLAG_BDONE), 1u, __ATOMIC_RELAXED, __HIP_MEMORY_SCOPE_AGENT);
    G2PHASE(acc, 1, 0, 3, 0, true);
    G2STEP(acc, 3);
    for (int k4 = 1; k4 < (NP - 1) / 4; k4++) { G2STEP(acc, 0); G2STEP(acc, 1); G2STEP(acc, 2); G2STEP(acc, 3); }
    constexpr int R0 = ((NP - 1) / 4) * 4;
    if (R0 + 0 < NP - 1) G2STEP(acc, 0);
    if (R0 + 1 < NP - 1) G2STEP(acc, 1);
    if (R0 + 2 < NP - 1) G2STEP(acc, 2);
    G2LAST(acc, (NP - 1) & 3);
    STAMP(3, 2);
    rsrc_t rp = mkrs((const char*)part + (size_t)pair * 65536, 65536);
    const unsigned po = wid * 16384 + lane * 16;
#pragma unroll
    for (int t = 0; t < 4; t++)
#pragma unroll
      for (int i = 0; i < 4; i++) __builtin_amdgcn_raw_buffer_store_b128(__builtin_bit_cast(u32x4, acc[t][i]), rp, po + (t * 4 + i) * 1024, 0, 16);
    WAITV(0); BAR();
    if (tid == 0) __hip_atomic_store((gu32*)(flags + FLAG_PAIR + 32 * pair), 1u, __ATOMIC_RELAXED, __HIP_MEMORY_SCOPE_AGENT);
    STAMP(3, 3);
    return;
  }
  float gg[4]; u32x2 bv[4];
#pragma unroll
  for (int i = 0; i < 4; i++) { gg[i] = g[256 * beta + alpha0 + 64 * wm + 16 * i + (lane & 15)]; bv[i] = *(const u32x2*)(b2 + d0 + 64 * wn + 16 * i + 4 * gq); }
  f32x4 aca[4][4];
#pragma unroll
  for (int t = 0; t < 4; t++)
#pragma unroll
    for (int i = 0; i < 4; i++) aca[t][i] = (f32x4){0.f, 0.f, 0.f, 0.f};
  unsigned pv = 0;
  auto poll_issue = [&](unsigned* fl) { if (wid == 0) asm volatile("global_load_dword %0, %1, %2 sc1" : "=v"(pv) : "v"(0u), "s"(fl) : "memory"); };
  auto poll_check = [&](unsigned* fl, unsigned want) {
    if (wid == 0) {
      asm volatile("s_waitcnt vmcnt(0)" : "+v"(pv) :: "memory");
      unsigned spins = 0, cur = __builtin_amdgcn_readfirstlane(pv);
      while (cur < want) { if (++spins > (1u << 22)) break; __builtin_amdgcn_s_sleep(1);
        cur = __builtin_amdgcn_readfirstlane(__hip_atomic_load((gu32*)fl, __ATOMIC_RELAXED, __HIP_MEMORY_SCOPE_AGENT)); }
      __builtin_amdgcn_fence(__ATOMIC_ACQUIRE, "agent");
    } };
  auto poll_done = [&]() { if (wid == 0) WAITV(0); };
  BAR();
  STAMP(3, 2);
#pragma unroll
  for (int n = 0; n < 12; n++) G2RDS(0, 0, 0, n);
  WAITL0();
  constexpr int NG = (NM - 6) / 4;
  for (int k4 = 0; k4 < NG; k4++) { G2STEP(acc, 0); G2STEP(acc, 1); G2STEP(acc, 2); G2STEP(acc, 3); }
#define G2STEPW(A, st, AW) do { G2PHASE(A, 0, 1, (st) & 3, 1, true); AW; BAR(); SB(); G2PHASE(A, 1, 0, ((st) + 1) & 3, 0, true); } while (0)
#pragma unroll
  for (int st = NG * 4; st < NM; st++) {
    if (st == NM - 6) G2STEPW(acc, st, poll_issue(flags + FLAG_BDONE));
    else if (st == NM - 5) G2STEPW(acc, st, poll_check(flags + FLAG_BDONE, 128u));
    else if (st == NM - 4) G2STEPW(acc, st, poll_done());
    else if (st == NM - 2) G2STEPW(acc, st, poll_issue(flags + FLAG_PAIR + 32 * pair));
    else if (st == NM - 1) G2STEPW(acc, st, poll_check(flags + FLAG_PAIR + 32 * pair, 1u));
    else G2STEP(acc, st & 3);
  }
  STAMP(3, 3);
#pragma unroll
  for (int st = NM; st < NF - 1; st++) {
    if (st == NM) G2STEPW(aca, st, poll_done());
    else G2STEP(aca, st & 3);
  }
  G2LAST(aca, (NF - 1) & 3);
  STAMP(3, 4);
  BAR();
  STAMP(3, 5);
  LAS char* pb = lds + ((wid & 1) ? G2_W0 : 0) + ((NM + (wid >> 1)) & 3) * G2_B + lane * 16;
#pragma unroll
  for (int t = 0; t < 4; t++) {
    const int dd = d0 + 64 * wn + 16 * t + 4 * gq;
    const float bb[4] = {__uint_as_float(bv[t][0] << 16), __uint_as_float(bv[t][0] & 0xffff0000u), __uint_as_float(bv[t][1] << 16), __uint_as_float(bv[t][1] & 0xffff0000u)};
#pragma unroll
    for (int i = 0; i < 4; i++) {
      const int al = alpha0 + 64 * wm + 16 * i + (lane & 15);
      const f32x4 pp = *(LAS f32x4*)(pb + (t * 4 + i) * 1024);
      const float og = rbf(1.f - gg[i]);
      float o[4];
#pragma unroll
      for (int r = 0; r < 4; r++) o[r] = gg[i] * aca[t][i][r] + og * (acc[t][i][r] + pp[r] + bb[r]);
      *(u32x2*)(out + (8 * al + beta) * D_ + dd) = (u32x2){pk2(o[0], o[1]), pk2(o[2], o[3])};
    }
  }
  STAMP(3, 6);
}

extern "C" void kernel_launch(void* const* d_in, const int* in_sizes, int n_in, void* d_out, int out_size, void* d_ws, size_t ws_size, hipStream_t stream) {
  const u16* x = (const u16*)d_in[0]; const u16* wr = (const u16*)d_in[1]; const u16* wi = (const u16*)d_in[2];
  const u16* w1 = (const u16*)d_in[4]; const u16* b1 = (const u16*)d_in[5]; const u16* w2 = (const u16*)d_in[6]; const u16* b2 = (const u16*)d_in[7];
  const u16* gw = (const u16*)d_in[8];
  char* ws = (char*)d_ws;
  float* g = (float*)ws;
  unsigned* flags = (unsigned*)(ws + (64 << 10));
  u16* T = (u16*)(ws + (1 << 20));
  u16* T2 = (u16*)(ws + (5 << 20));
  u16* h = (u16*)(ws + (9 << 20));
  float* part = (float*)(ws + (24 << 20));
  (void)hipFuncSetAttribute((const void*)k_gemm1, hipFuncAttributeMaxDynamicSharedMemorySize, G1_LDS);
  (void)hipFuncSetAttribute((const void*)k_gemm2, hipFuncAttributeMaxDynamicSharedMemorySize, G2_LDS);
  k_gemm1<<<256, 512, G1_LDS, stream>>>(x, w1, b1, gw, h, T, g, flags);
  k_gemm2<<<256, 512, G2_LDS, stream>>>(h, w2, b2, T, wr, wi, T2, g, (u16*)d_out, part, flags);
}
```

```cpp
#define PF1_SLEEP 0
#include <hip/hip_runtime.h>
#include <stdint.h>
typedef unsigned short u16;
#define LAS __attribute__((address_space(3)))
#define DI __device__ __forceinline__
typedef short s16x4 __attribute__((ext_vector_type(4)));
typedef short s16x8 __attribute__((ext_vector_type(8)));
typedef __bf16 bf16x8 __attribute__((ext_vector_type(8)));
typedef __bf16 bf16x2 __attribute__((ext_vector_type(2)));
typedef float f32x2 __attribute__((ext_vector_type(2)));
typedef float f32x4 __attribute__((ext_vector_type(4)));
typedef float f32x16 __attribute__((ext_vector_type(16)));
typedef unsigned u32x2 __attribute__((ext_vector_type(2)));
typedef unsigned u32x4 __attribute__((ext_vector_type(4)));
typedef __amdgpu_buffer_rsrc_t rsrc_t;

constexpr int S_ = 2048, D_ = 1024, F_ = 3584, DF_ = 513;

namespace ct {
constexpr double PI = 3.14159265358979323846;
constexpr double ccos(double x) { double x2 = x * x, t = 1, s = 1; for (int i = 1; i < 16; i++) { t *= -x2 / ((2 * i - 1) * (2 * i)); s += t; } return s; }
constexpr double cos256(int m) { m &= 255; if (m > 128) m = 256 - m; bool neg = false; if (m > 64) { m = 128 - m; neg = true; } double v = (m == 64) ? 0.0 : ccos(2 * PI * m / 256.0); return neg ? -v : v; }
constexpr u16 tobf(double d) { float f = (float)d; unsigned u = __builtin_bit_cast(unsigned, f); u += 0x7fffu + ((u >> 16) & 1u); return (u16)(u >> 16); }
struct Tab { u16 c[256]; };
constexpr Tab mktab() { Tab t{}; for (int i = 0; i < 256; i++) t.c[i] = tobf(cos256(i)); return t; }
constexpr Tab COS = mktab();
constexpr u16 E(int dp, int a) { if (dp == 0) return COS.c[0]; if (dp == 1) return COS.c[(128 * a) & 255];
  int dl = dp >> 1; int m = (dl * a) & 255; return (dp & 1) ? COS.c[(m + 64) & 255] : COS.c[m]; }
struct Chunk { u16 v[8192]; };
template <bool TR> constexpr Chunk mk(int ch) { Chunk c{}; for (int i = 0; i < 8192; i++) { int j = i & 7, lane = (i >> 3) & 63, kb = (i >> 9) & 7, blk = ch * 2 + (i >> 12);
  int col = blk * 16 + (lane & 15); int k = 32 * kb + 16 * (j >> 2) + 4 * (lane >> 4) + (j & 3); c.v[i] = TR ? E(k, col) : E(col, k); } return c; }
template <bool TR, int I> constexpr Chunk CH = mk<TR>(I);
struct Big { Chunk c[8]; };
}
__device__ __attribute__((aligned(1024))) const ct::Big ETAB1 = {{ct::CH<false,0>, ct::CH<false,1>, ct::CH<false,2>, ct::CH<false,3>, ct::CH<false,4>, ct::CH<false,5>, ct::CH<false,6>, ct::CH<false,7>}};
__device__ __attribute__((aligned(1024))) const ct::Big ETAB2 = {{ct::CH<true,0>, ct::CH<true,1>, ct::CH<true,2>, ct::CH<true,3>, ct::CH<true,4>, ct::CH<true,5>, ct::CH<true,6>, ct::CH<true,7>}};

namespace ct {
constexpr double cosq(int m, int n) {
  m %= n; if (m < 0) m += n; if (2 * m > n) m = n - m; bool neg = false; if (4 * m > n) { m = n / 2 - m; neg = true; }
  double v = (4 * m == n) ? 0.0 : ((8 * m > n) ? 0.0 : 0.0);
  if (4 * m != n) { double x = 2 * PI * m / n; v = ccos(x); }
  return neg ? -v : v; }
struct C2048 { double c[513]; };
constexpr C2048 mk2048() { C2048 t{}; for (int i = 0; i <= 512; i++) t.c[i] = (i == 512) ? 0.0 : ccos(2 * PI * i / 2048.0); return t; }
constexpr C2048 CQ = mk2048();
constexpr double c2048(int m) { m &= 2047; if (m > 1024) m = 2048 - m; return m > 512 ? -CQ.c[1024 - m] : CQ.c[m]; }
constexpr double s2048(int m) { return c2048(m - 512); }
constexpr unsigned f2u(double d) { return __builtin_bit_cast(unsigned, (float)d); }
constexpr int crow_(int r, int h) { return (r & 3) + 8 * (r >> 2) + 4 * h; }
struct CTab { unsigned v[12 * 64 * 4]; };
constexpr CTab mkctab() { CTab t{};
  for (int lane = 0; lane < 64; lane++) { const int l31 = lane & 31, hh = lane >> 5;
    for (int s = 0; s < 2; s++) for (int e = 0; e < 4; e++) { const int k0 = crow_(8 * s + 2 * e, hh), k1 = crow_(8 * s + 2 * e + 1, hh);
      t.v[((0 + s) * 64 + lane) * 4 + e] = (unsigned)tobf(c2048(64 * l31 * k0)) | ((unsigned)tobf(c2048(64 * l31 * k1)) << 16);
      t.v[((2 + s) * 64 + lane) * 4 + e] = (unsigned)tobf(s2048(64 * l31 * k0)) | ((unsigned)tobf(s2048(64 * l31 * k1)) << 16); }
    for (int r = 0; r < 16; r++) { t.v[((4 + (r >> 2)) * 64 + lane) * 4 + (r & 3)] = f2u(c2048(2 * crow_(r, hh) * l31)); t.v[((8 + (r >> 2)) * 64 + lane) * 4 + (r & 3)] = f2u(s2048(2 * crow_(r, hh) * l31)); } }
  return t; }
struct MChunk { unsigned v[8 * 64 * 4]; };
constexpr int jp_(bool sp, int dl, int m) { return sp ? (m < 2 ? 1024 * m : (m < 8 ? 256 * (m >> 1) : 128 + 256 * ((m - 8) >> 1))) : dl + 256 * (m >> 1); }
constexpr MChunk mkm(int ch) { MChunk t{};
  for (int u = 0; u < 8; u++) for (int lane = 0; lane < 64; lane++) { const int dl = ch * 8 + u; const bool sp = dl == 0; const int n = lane & 15, g = lane >> 4; u16 f[4] = {}, iv[4] = {};
    for (int j = 0; j < 4; j++) { const int k = 4 * g + j; const bool ok = !sp || ((n < 8) == (k < 8));
      { const int a = (sp ? (k & 7) : (k >> 1)) * jp_(sp, dl, n); const bool ii = !sp && (k & 1), io = (n & 1) && (!sp || n >= 2);
        const double v = (ii == io) ? c2048(a) : (ii ? s2048(a) : -s2048(a)); f[j] = tobf(ok ? v : 0.0); }
      { const int a = (sp ? (n & 7) : (n >> 1)) * jp_(sp, dl, k); const bool ii = (k & 1) && (!sp || k >= 2), io = !sp && (n & 1);
        const double v = (ii == io) ? c2048(a) : (ii ? -s2048(a) : s2048(a)); iv[j] = tobf(ok ? v * ((sp && k < 2 ? 1.0 : 2.0) / 2097152.0) : 0.0); } }
    unsigned* o = &t.v[(u * 64 + lane) * 4]; o[0] = f[0] | ((unsigned)f[1] << 16); o[1] = f[2] | ((unsigned)f[3] << 16); o[2] = iv[0] | ((unsigned)iv[1] << 16); o[3] = iv[2] | ((unsigned)iv[3] << 16); }
  return t; }
template <int I> constexpr MChunk MCH = mkm(I);
struct MBig { MChunk c[16]; };
}
__device__ __attribute__((aligned(1024))) const ct::CTab CTAB = ct::mkctab();
__device__ __attribute__((aligned(1024))) const ct::MBig MTAB = {{ct::MCH<0>, ct::MCH<1>, ct::MCH<2>, ct::MCH<3>, ct::MCH<4>, ct::MCH<5>, ct::MCH<6>, ct::MCH<7>, ct::MCH<8>, ct::MCH<9>, ct::MCH<10>, ct::MCH<11>, ct::MCH<12>, ct::MCH<13>, ct::MCH<14>, ct::MCH<15>}};

DI float bf2f(u16 v) { return __uint_as_float(((unsigned)v) << 16); }
DI unsigned pk2(float lo, float hi) { f32x2 v = {lo, hi}; bf16x2 b = __builtin_convertvector(v, bf16x2); return __builtin_bit_cast(unsigned, b); }
DI float rbf(float f) { return __uint_as_float(pk2(f, 0.f) << 16); }
DI rsrc_t mkrs(const void* p, unsigned bytes) { return __builtin_amdgcn_make_buffer_rsrc((void*)p, 0, bytes, 0x00020000); }
DI void dma16(rsrc_t rs, LAS char* dst, unsigned voff, unsigned soff) {
  asm volatile("s_mov_b32 m0, %0\n\ts_nop 0\n\tbuffer_load_dwordx4 %1, %2, %3 offen lds" :: "s"((unsigned)(size_t)dst), "v"(voff), "s"(rs), "s"(soff) : "memory");
}
DI u32x4 gld16(const void* base, unsigned voff) { u32x4 r; asm volatile("global_load_dwordx4 %0, %1, %2" : "=v"(r) : "v"(voff), "s"(base) : "memory"); return r; }
DI unsigned gld2(const void* base, unsigned voff) { unsigned r; asm volatile("global_load_ushort %0, %1, %2" : "=v"(r) : "v"(voff), "s"(base) : "memory"); return r; }
DI s16x4 trd(LAS char* p) { return __builtin_amdgcn_ds_read_tr16_b64_v4i16((LAS s16x4*)p); }
DI bf16x8 cat8(s16x4 a, s16x4 b) { return __builtin_bit_cast(bf16x8, __builtin_shufflevector(a, b, 0, 1, 2, 3, 4, 5, 6, 7)); }
DI bf16x8 ld128(LAS char* p) { return *(LAS bf16x8*)p; }
#define MFMA16(a, b, c) __builtin_amdgcn_mfma_f32_16x16x32_bf16((a), (b), (c), 0, 0, 0)
#define MFMA32(a, b, c) __builtin_amdgcn_mfma_f32_32x32x16_bf16((a), (b), (c), 0, 0, 0)
#define WAITV(n) asm volatile("s_waitcnt vmcnt(" #n ")" ::: "memory")
#define WAITL(n) asm volatile("s_waitcnt lgkmcnt(" #n ")" ::: "memory")
#define WAITL0() __builtin_amdgcn_s_waitcnt(0xC07F)
#define BAR() __builtin_amdgcn_s_barrier()

DI unsigned pk2v(f32x2 v) { bf16x2 b = __builtin_convertvector(v, bf16x2); return __builtin_bit_cast(unsigned, b); }
DI unsigned gelu2(float a, float b) {
  const float c1 = -2.302208198f, c2 = -0.1029432397f;
  f32x2 v = {a, b};
  f32x2 t = v * v; f32x2 p = t * c2 + c1; f32x2 z = v * p;
  f32x2 e; e.x = __builtin_amdgcn_exp2f(z.x); e.y = __builtin_amdgcn_exp2f(z.y);
  f32x2 d = e + 1.f; f32x2 r; r.x = __builtin_amdgcn_rcpf(d.x); r.y = __builtin_amdgcn_rcpf(d.y);
  return pk2v(v * r);
}
DI float gelu_t(float v) {
  const float c1 = -2.302208198f, c2 = -0.1029432397f;
  float z = v * (c1 + c2 * v * v);
  float e = __builtin_amdgcn_exp2f(z);
  return v * __builtin_amdgcn_rcpf(1.f + e);
}

#define WST(i)
#define STAMP(k, i)

#define SB() __builtin_amdgcn_sched_barrier(0)
typedef __attribute__((address_space(1))) unsigned gu32;
constexpr int FLAG_BDONE = 0;
constexpr int FLAG_PAIR = 32;

constexpr int G1_XB = 16384, G1_WB = 32768, G1_W0 = 3 * G1_XB, G1_BI = G1_W0 + 3 * G1_WB, G1_CNT = G1_BI + 1024, G1_LDS = G1_CNT + 16;
DI void touch(unsigned& dummy, const void* p) { asm volatile("global_load_dword %0, %1, off" : "+v"(dummy) : "v"(p) : "memory"); }
__global__ __launch_bounds__(512, 2) void k_gemm1(const u16* __restrict__ x, const u16* __restrict__ w1, const u16* __restrict__ b1, const u16* __restrict__ gw,
                                                  u16* __restrict__ h, u16* __restrict__ T, float* __restrict__ g, unsigned* __restrict__ flags,
                                                  const u16* __restrict__ w2, const u16* __restrict__ wr, const u16* __restrict__ wi) {
  extern __shared__ __attribute__((aligned(16))) char smem[];
  LAS char* lds = (LAS char*)smem;
  const int tid = threadIdx.x, lane = tid & 63, wid = __builtin_amdgcn_readfirstlane(tid >> 6);
  const int b = blockIdx.x, xcd = b & 7, idx = b >> 3;
  const int mb = (xcd & 1) * 8 + (idx & 7), nb = (xcd >> 1) * 4 + (idx >> 3);
  STAMP(1, 0);
  if (wid >= 4) {
    const int lw = wid - 4;
    {
      const int beta = mb >> 1, alpha0 = (mb & 1) * 128;
      const unsigned xo = (8 * alpha0 + beta) * D_;
      const unsigned wo = (nb * 448) & ~127u;
      rsrc_t rx = mkrs(x + xo, (S_ * D_ - xo) * 2);
      rsrc_t rw = mkrs((const char*)w1 + wo, D_ * F_ * 2 - wo);
      rsrc_t rb = mkrs(b1 + nb * 224, 448);
      unsigned vx[2], vw[8];
      { const int r = lane >> 3, c1 = lane & 7;
        vx[0] = r * 16384 + ((c1 ^ (r >> 1)) * 16); vx[1] = r * 16384 + ((c1 ^ (4 + (r >> 1))) * 16); }
#pragma unroll
      for (int i = 0; i < 8; i++) { const int hi = lane >> 5, k = 2 * i + hi, f = (k & 3) | (((k >> 3) & 1) << 2);
        vw[i] = hi * (F_ * 2) + (((lane & 31) ^ (f << 1)) * 16); }
      auto stage = [&](int kt, int buf) {
#pragma unroll
        for (int i = 0; i < 4; i++) { const int j = lw * 4 + i; dma16(rx, lds + buf * G1_XB + j * 1024, vx[i & 1], j * 8 * 16384 + kt * 128); }
#pragma unroll
        for (int i = 0; i < 8; i++) { const int j = lw * 8 + i; dma16(rw, lds + G1_W0 + buf * G1_WB + j * 1024, vw[i], (kt * 64 + 2 * j) * (F_ * 2)); }
      };
      if (lw == 0) dma16(rb, lds + G1_BI, lane * 16, 0);
      stage(0, 0); stage(1, 1); stage(2, 2);
      if (lw == 1) {
        if (lane == 0) *(LAS unsigned*)(lds + G1_CNT) = 0u;
        if (b < 129 && lane == 0) flags[b * 32] = 0u;
      }
      WAITV(24); WAITL(0); BAR();
      int cb = 0;
      for (int kt = 0; kt < 15; kt++) {
        if (kt < 14) WAITV(12); else WAITV(0);
        BAR();
        if (kt + 3 < 16) stage(kt + 3, cb);
        cb = (cb == 2) ? 0 : cb + 1;
      }
    }
    const int beta = b & 7, dblk = (b >> 3) & 15, dh = b >> 7;
    LAS char* tl = lds + G1_W0 + G1_WB;
    {
      rsrc_t rx = mkrs(x + beta * D_ + dblk * 64, (unsigned)(S_ * D_ * 2 - (beta * D_ + dblk * 64) * 2));
      const int hi = lane >> 5, r = (lane & 31) >> 2, c = (lane & 3) ^ (2 * ((r >> 2) & 1));
      const unsigned voff = r * 16384 + hi * 64 + c * 16;
#pragma unroll
      for (int i = 0; i < 8; i++) { const int j = lw * 8 + i; dma16(rx, tl + j * 1024, voff, j * 8 * 16384); }
    }
    bf16x8 ef[2][8];
    {
      const u32x4* et = (const u32x4*)&ETAB1;
#pragma unroll
      for (int c = 0; c < 2; c++)
#pragma unroll
        for (int kb = 0; kb < 8; kb++) ef[c][kb] = __builtin_bit_cast(bf16x8, et[((dh * 8 + lw * 2 + c) * 8 + kb) * 64 + lane]);
    }
    {
      const u32x4* gp = (const u32x4*)gw;
      u32x4 g0 = gp[lane * 2], g1 = gp[lane * 2 + 1];
#pragma unroll
      for (int rr = 0; rr < 2; rr++) {
        const int s = b * 8 + lw * 2 + rr;
        const u32x4* xp = (const u32x4*)(x + s * D_);
        u32x4 x0 = xp[lane * 2], x1 = xp[lane * 2 + 1];
        float acc = 0.f;
#pragma unroll
        for (int e = 0; e < 4; e++) {
          acc += rbf(__uint_as_float(x0[e] << 16) * __uint_as_float(g0[e] << 16));
          acc += rbf(__uint_as_float(x0[e] & 0xffff0000u) * __uint_as_float(g0[e] & 0xffff0000u));
          acc += rbf(__uint_as_float(x1[e] << 16) * __uint_as_float(g1[e] << 16));
          acc += rbf(__uint_as_float(x1[e] & 0xffff0000u) * __uint_as_float(g1[e] & 0xffff0000u));
        }
#pragma unroll
        for (int o = 32; o > 0; o >>= 1) acc += __shfl_xor(acc, o);
        if (lane == 0) { float z = rbf(acc); g[256 * (s & 7) + (s >> 3)] = rbf(1.f / (1.f + __expf(-z))); }
      }
    }
    WAITV(0);
    if (lane == 0) __hip_atomic_fetch_add((LAS unsigned*)(lds + G1_CNT), 1u, __ATOMIC_RELAXED, __HIP_MEMORY_SCOPE_WORKGROUP);
    while (__builtin_amdgcn_readfirstlane(*(volatile LAS unsigned*)(lds + G1_CNT)) < 4u) __builtin_amdgcn_s_sleep(1);
    asm volatile("" ::: "memory");
    const int gq = lane >> 4, q = (lane & 15) >> 2, p = lane & 3;
    f32x4 acc[2][4];
#pragma unroll
    for (int c = 0; c < 2; c++)
#pragma unroll
      for (int i = 0; i < 4; i++) acc[c][i] = (f32x4){0.f, 0.f, 0.f, 0.f};
    LAS char* base = tl + (gq >> 1) * 2 * 512 + (4 * (gq & 1) + q) * 64 + ((p ^ (2 * (gq & 1))) * 16);
#pragma unroll
    for (int kb = 0; kb < 8; kb++) {
#pragma unroll
      for (int i = 0; i < 4; i++) {
        LAS char* a = base + (4 * kb) * 1024 + (i >> 1) * 512 + (i & 1) * 8;
        bf16x8 A = cat8(trd(a), trd(a + 2 * 1024));
#pragma unroll
        for (int c = 0; c < 2; c++) acc[c][i] = MFMA16(A, ef[c][kb], acc[c][i]);
      }
    }
#pragma unroll
    for (int c = 0; c < 2; c++) {
      const int dp = dh * 128 + lw * 32 + c * 16 + (lane & 15);
#pragma unroll
      for (int ip = 0; ip < 2; ip++) {
        u32x4 w;
        w[0] = pk2(acc[c][2 * ip][0], acc[c][2 * ip][1]); w[1] = pk2(acc[c][2 * ip][2], acc[c][2 * ip][3]);
        w[2] = pk2(acc[c][2 * ip + 1][0], acc[c][2 * ip + 1][1]); w[3] = pk2(acc[c][2 * ip + 1][2], acc[c][2 * ip + 1][3]);
        *(u32x4*)(T + (beta * 256 + dp) * D_ + dblk * 64 + ip * 32 + gq * 8) = w;
      }
    }
    {
      unsigned dummy = 0; const int gwv = b * 4 + lw;
      touch(dummy, (const char*)w2 + (size_t)(gwv * 56 + (lane < 56 ? lane : 0)) * 128);
      const char* p2;
      { const int l = lane & 31; int li = gwv * 17 + (l < 17 ? l : 0); if (li > 16415) li = 16415;
        p2 = (const char*)(lane < 32 ? wr : wi) + (size_t)li * 128;
        if (l == 17) p2 = (const char*)&ETAB2 + gwv * 128;
        if (l == 18) p2 = (const char*)&MTAB + gwv * 128;
        if (l == 19) p2 = (const char*)&CTAB + (gwv % 96) * 128; }
      touch(dummy, p2);
      asm volatile("s_waitcnt vmcnt(0)" : "+v"(dummy) :: "memory");
    }
    return;
  }
  const int wm = wid >> 1, wn = wid & 1;
  const int c = lane & 15, gq = lane >> 4, q = (lane & 15) >> 2, p = lane & 3;
  LAS char* xa0 = lds + (64 * wm + c) * 128 + ((gq ^ (c >> 1)) * 16);
  LAS char* xa1 = lds + (((64 * wm + c) * 128 + ((gq ^ (c >> 1)) * 16)) ^ 64);
  const unsigned wa0 = G1_W0 + (8 * gq + q) * 512 + ((q | ((gq & 1) << 2)) << 5) + ((p >> 1) << 4) + ((p & 1) << 3);
  const unsigned tsh = (7 * wn + 2 * (nb & 1));
  unsigned wa[7];
#pragma unroll
  for (int t = 0; t < 7; t++) wa[t] = wa0 ^ ((tsh + t) << 5);
  f32x4 acc[7][4];
  bf16x8 xf[2][4];
  s16x4 wl[2][7], wh[2][7];
  unsigned pfd = 0;
  { if (PF1_SLEEP > 0) __builtin_amdgcn_s_sleep(PF1_SLEEP);
    const int gwv = b * 4 + wid;
    const char* p1; const char* p2;
    {
      const int e = gwv * 17 + lane;
      const char* pe = e < 6144 ? (const char*)x + (size_t)(e / 3) * 2048 + (e % 3) * 128 : (const char*)w1 + (size_t)((e - 6144) < 10752 ? (e - 6144) : 0) * 128;
      const int lx = gwv * 26 + (lane - 17);
      const char* px = (const char*)x + (size_t)(lx / 13) * 2048 + (3 + lx % 13) * 128;
      const int lw_ = gwv * 46 + (lane - 43);
      const char* pw = (const char*)w1 + (size_t)(10752 + (lw_ < 46592 ? lw_ : 0)) * 128;
      p1 = lane < 17 ? pe : (lane < 43 ? px : pw);
      const int l2 = gwv * 46 + 21 + (lane < 25 ? lane : 0);
      p2 = (const char*)w1 + (size_t)(10752 + (l2 < 46592 ? l2 : 0)) * 128; }
    touch(pfd, p1); touch(pfd, p2); }
  BAR();
  STAMP(1, 1);
#pragma unroll
  for (int t = 0; t < 7; t++) { u32x2 bv = *(LAS u32x2*)(lds + G1_BI + (16 * (7 * wn + t) + 4 * gq) * 2);
    f32x4 bb = {__uint_as_float(bv[0] << 16), __uint_as_float(bv[0] & 0xffff0000u), __uint_as_float(bv[1] << 16), __uint_as_float(bv[1] & 0xffff0000u)};
#pragma unroll
    for (int i = 0; i < 4; i++) acc[t][i] = bb; }
#define RDX(set, buf, ks, i) xf[set][i] = ld128(((ks) ? xa1 : xa0) + (buf) * G1_XB + (i) * 2048)
#define RDWL(set, buf, ks, t) wl[set][t] = trd(lds + wa[t] + ((buf) & 1) * G1_WB + (ks) * 16384)
#define RDWH(set, buf, ks, t) wh[set][t] = trd(lds + wa[t] + ((buf) & 1) * G1_WB + (ks) * 16384 + 2048)
#define MM(set, t, i) acc[t][i] = MFMA16(cat8(wl[set][t], wh[set][t]), xf[set][i], acc[t][i])
#define RDS(ns, buf, ks, n) do { if ((n) < 4) RDX(ns, buf, ks, (n) & 3); else if ((n) < 18) { if (((n) & 1) == 0) RDWL(ns, buf, ks, (((n) - 4) >> 1) % 7); else RDWH(ns, buf, ks, (((n) - 4) >> 1) % 7); } } while (0)
#define PHASE(cs, ns, buf, ks, DO_RD, ADJ) do { \
    _Pragma("unroll") for (int s_ = 0; s_ < 28; s_++) { MM(cs, s_ >> 2, s_ & 3); if (DO_RD) RDS(ns, buf, ks, s_); \
      if ((ADJ) != 0 && s_ >= 18 && s_ < 25) { wa[(s_ - 18) % 7] += (ADJ); asm volatile("" : "+v"(wa[(s_ - 18) % 7])); } SB(); } \
    WAITL0(); } while (0)
#pragma unroll
  for (int n = 0; n < 18; n++) RDS(0, 0, 0, n);
  WAITL0();
  for (int k3 = 0; k3 < 5; k3++) {
    PHASE(0, 1, 0, 1, true, 0); BAR(); SB(); PHASE(1, 0, 1, 0, true, 0);
    PHASE(0, 1, 1, 1, true, 2 * G1_WB); BAR(); SB(); PHASE(1, 0, 2, 0, true, 0);
    PHASE(0, 1, 2, 1, true, -2 * G1_WB); BAR(); SB(); PHASE(1, 0, 0, 0, true, 0);
  }
  PHASE(0, 1, 0, 1, true, 0);
  PHASE(1, 0, 0, 0, false, 0);
  asm volatile("s_waitcnt vmcnt(0)" : "+v"(pfd) :: "memory");
  STAMP(1, 2);
#pragma unroll
  for (int i = 0; i < 4; i++) {
    char* hb = (char*)h + ((size_t)(8 * mb + 4 * wm + i) * 112 + 7 * nb) * 1024 + lane * 16;
    unsigned pk[7][2];
#pragma unroll
    for (int t = 0; t < 7; t++) { pk[t][0] = gelu2(acc[t][i][0], acc[t][i][1]); pk[t][1] = gelu2(acc[t][i][2], acc[t][i][3]); }
    if (wn == 0) {
#pragma unroll
      for (int pr = 0; pr < 3; pr++) *(u32x4*)(hb + pr * 1024) = (u32x4){pk[2 * pr][0], pk[2 * pr][1], pk[2 * pr + 1][0], pk[2 * pr + 1][1]};
      *(u32x2*)(hb + 3 * 1024) = (u32x2){pk[6][0], pk[6][1]};
    } else {
      *(u32x2*)(hb + 3 * 1024 + 8) = (u32x2){pk[0][0], pk[0][1]};
#pragma unroll
      for (int pr = 0; pr < 3; pr++) *(u32x4*)(hb + (4 + pr) * 1024) = (u32x4){pk[2 * pr + 1][0], pk[2 * pr + 1][1], pk[2 * pr + 2][0], pk[2 * pr + 2][1]};
    }
  }
  STAMP(1, 3);
}

DI int crow(int r, int h) { return (r & 3) + 8 * (r >> 2) + 4 * h; }
DI bf16x8 pack8(const f32x16& v, int s) { u32x4 w; w[0] = pk2(v[8 * s], v[8 * s + 1]); w[1] = pk2(v[8 * s + 2], v[8 * s + 3]); w[2] = pk2(v[8 * s + 4], v[8 * s + 5]); w[3] = pk2(v[8 * s + 6], v[8 * s + 7]); return __builtin_bit_cast(bf16x8, w); }
DI unsigned ploff(int pl, unsigned bb) { return pl * 2048 + (bb ^ ((pl & 7) << 5)); }
DI unsigned ploff2(int pl, unsigned bb) { return pl * 2048 + (bb ^ (pl << 3)); }
DI unsigned ploff3(int pl, unsigned bb) { return pl * 2048 + (bb ^ (((bb >> 7) & 7) << 3) ^ ((pl & 1) << 3) ^ (((pl >> 1) & 3) << 6)); }
typedef unsigned u32x4u __attribute__((ext_vector_type(4), aligned(2)));
DI unsigned swp16(unsigned v) { return __builtin_amdgcn_alignbit(v, v, 16); }
DI float bfe(s16x4 v, int e) { return __uint_as_float(((unsigned)(u16)v[e]) << 16); }
struct cplx16 { f32x16 re, im; };
struct frag2 { bf16x8 r[2], i[2]; };
DI frag2 packc(const cplx16& c) { frag2 f; f.r[0] = pack8(c.re, 0); f.r[1] = pack8(c.re, 1); f.i[0] = pack8(c.im, 0); f.i[1] = pack8(c.im, 1); return f; }
template <class F>
DI void spectral_unit(LAS char* pls, LAS char* fs_own, LAS char* cst, const int dl, const int wid, const int lane, const u16* __restrict__ T, const u16* __restrict__ wr, const u16* __restrict__ wi, u16* __restrict__ T2, F&& dmaq) {
  STAMP(2, 0); WST(0);
  const bool sp = (dl == 0);
  const bool packed = sp && wid == 0;
  const int l31 = lane & 31, hh = lane >> 5;
  struct raw_f { u32x4 v[4]; unsigned x[4]; };
  struct filt { s16x4 re[4], im[4]; float x[4]; };
  auto filter_issue = [&](int j) { raw_f w; const int jm = (2048 - j) & 2047;
    const unsigned od = (j * DF_ + 8 * lane) * 2, om = (jm * DF_ + 8 * lane + 1) * 2;
    w.v[0] = gld16(wr, od); w.v[1] = gld16(wi, od); w.v[2] = gld16(wr, om); w.v[3] = gld16(wi, om);
    w.x[0] = gld2(wr, jm * DF_ * 2); w.x[1] = gld2(wi, jm * DF_ * 2); w.x[2] = gld2(wr, (j * DF_ + 512) * 2); w.x[3] = gld2(wi, (j * DF_ + 512) * 2);
    return w; };
  auto filter_stage = [&](LAS char* fs, const raw_f& w) { filt f;
    *(LAS u32x4*)(fs + lane * 16) = w.v[0];
    *(LAS u32x4*)(fs + 2048 + lane * 16) = w.v[1];
    *(LAS u32x4*)(fs + 2032 - lane * 16) = (u32x4){swp16(w.v[2][3]), swp16(w.v[2][2]), swp16(w.v[2][1]), swp16(w.v[2][0])};
    *(LAS u32x4*)(fs + 2048 + 2032 - lane * 16) = (u32x4){swp16(w.v[3][3]), swp16(w.v[3][2]), swp16(w.v[3][1]), swp16(w.v[3][0])};
    asm volatile("" ::: "memory");
    const int blk = (lane >> 4) & 1, q = (lane & 15) >> 2, p = lane & 3;
#pragma unroll
    for (int G = 0; G < 4; G++) { const unsigned off = (8 * G + 4 * hh + q) * 64 + (16 * blk + 4 * p) * 2; f.re[G] = trd(fs + off); f.im[G] = trd(fs + 2048 + off); }
#pragma unroll
    for (int i = 0; i < 4; i++) f.x[i] = __uint_as_float(w.x[i] << 16);
    return f; };
  {
    rsrc_t rt = mkrs(T, 8 * 256 * 1024 * 2);
#pragma unroll
    for (int i = 0; i < 4; i++) { const int j = wid * 4 + i, pl = j >> 1, hf = j & 1;
      const int row = sp ? ((pl & 7) * 256 + (pl >> 3)) : ((pl >> 1) * 256 + 2 * dl + (pl & 1));
      dma16(rt, pls + pl * 2048 + hf * 1024, (lane ^ ((pl & 7) << 1)) * 16, row * 2048 + hf * 1024); }
  }
  const int jrow = sp ? (wid < 4 ? 256 * wid : 128 + 256 * (wid - 4)) : dl + 256 * wid;
  raw_f rwA = filter_issue(jrow), rwC;
  if (packed) rwC = filter_issue(1024);
  {
    rsrc_t rc = mkrs(&CTAB, 12 * 1024), rm = mkrs((const char*)&MTAB + dl * 1024, 1024);
    dma16(rc, cst + wid * 1024, lane * 16, wid * 1024);
    if (wid < 4) dma16(rc, cst + (8 + wid) * 1024, lane * 16, (8 + wid) * 1024);
    else dma16(rm, cst + 12 * 1024, lane * 16, 0);
  }
  dmaq(0); dmaq(1);
  WST(1);
  asm volatile("s_waitcnt vmcnt(8)" : "+v"(rwA.v[0]), "+v"(rwA.v[1]), "+v"(rwA.v[2]), "+v"(rwA.v[3]), "+v"(rwA.x[0]), "+v"(rwA.x[1]), "+v"(rwA.x[2]), "+v"(rwA.x[3]) :: "memory");
  if (packed) asm volatile("" : "+v"(rwC.v[0]), "+v"(rwC.v[1]), "+v"(rwC.v[2]), "+v"(rwC.v[3]), "+v"(rwC.x[0]), "+v"(rwC.x[1]), "+v"(rwC.x[2]), "+v"(rwC.x[3]) :: "memory");
  WST(3);
  filt fA = filter_stage(fs_own, rwA);
  WAITL0();
  WST(4);
  BAR();
  WST(5);
  STAMP(2, 1);
  u32x4 ct_[12], mt_;
  mt_ = *(LAS u32x4*)(cst + 12 * 1024 + lane * 16);
#pragma unroll
  for (int i = 0; i < 12; i++) ct_[i] = *(LAS u32x4*)(cst + i * 1024 + lane * 16);
  const s16x4 Mf = __builtin_bit_cast(s16x4, (u32x2){mt_[0], mt_[1]}), Mi = __builtin_bit_cast(s16x4, (u32x2){mt_[2], mt_[3]});
  bf16x8 Cc[2], Sn[2], nS[2];
#pragma unroll
  for (int s = 0; s < 2; s++) { Cc[s] = __builtin_bit_cast(bf16x8, ct_[s]); Sn[s] = __builtin_bit_cast(bf16x8, ct_[2 + s]); nS[s] = __builtin_bit_cast(bf16x8, ct_[2 + s] ^ 0x80008000u); }
  f32x2 twc[8], tws[8];
#pragma unroll
  for (int r = 0; r < 8; r++) { twc[r] = (f32x2){__uint_as_float(ct_[4 + (r >> 1)][(2 * r) & 3]), __uint_as_float(ct_[4 + (r >> 1)][(2 * r + 1) & 3])};
                                tws[r] = (f32x2){__uint_as_float(ct_[8 + (r >> 1)][(2 * r) & 3]), __uint_as_float(ct_[8 + (r >> 1)][(2 * r + 1) & 3])}; }
  { int ln = lane; asm volatile("" : "+v"(ln)); const int g = ln >> 4, q = (ln & 15) >> 2, p = ln & 3, n = ln & 15;
    s16x4 a[8];
#pragma unroll
    for (int t = 0; t < 8; t++) a[t] = trd(pls + ploff(4 * g + q, (wid * 128 + t * 16 + 4 * p) * 2));
#pragma unroll
    for (int t = 0; t < 8; t++) {
      f32x4 dd = __builtin_amdgcn_mfma_f32_16x16x16bf16_1k(a[t], Mf, (f32x4){0.f, 0.f, 0.f, 0.f}, 0, 0, 0);
      *(LAS u32x2*)(pls + ploff2(n, (wid * 128 + t * 16 + 4 * g) * 2)) = (u32x2){pk2(dd[0], dd[1]), pk2(dd[2], dd[3])}; } }
  WAITL0(); WST(6); BAR(); WST(7);
  STAMP(2, 2);
  {
    const f32x16 z = {0.f};
    int ln0 = lane; asm volatile("" : "+v"(ln0));
    const int blk = (ln0 >> 4) & 1, q = (ln0 & 15) >> 2, p = ln0 & 3;
    const int pA = 2 * wid;
    frag2 X;
    { const int hh = ln0 >> 5;
#pragma unroll
      for (int s = 0; s < 2; s++) { const unsigned off = (16 * s + 4 * hh + q) * 64 + (16 * blk + 4 * p) * 2;
        X.r[s] = cat8(trd(pls + ploff2(pA, off)), trd(pls + ploff2(pA, off + 512)));
        X.i[s] = cat8(trd(pls + ploff2(pA + 1, off)), trd(pls + ploff2(pA + 1, off + 512))); } }
    auto stA = [&](const frag2& X, bool inv) { cplx16 c; const bf16x8* P = inv ? nS : Sn; const bf16x8* N = inv ? Sn : nS;
      c.re = MFMA32(X.r[0], Cc[0], z); c.im = MFMA32(X.i[0], Cc[0], z); c.re = MFMA32(X.r[1], Cc[1], c.re); c.im = MFMA32(X.i[1], Cc[1], c.im);
      c.re = MFMA32(X.i[0], P[0], c.re); c.im = MFMA32(X.r[0], N[0], c.im); c.re = MFMA32(X.i[1], P[1], c.re); c.im = MFMA32(X.r[1], N[1], c.im); return c; };
    auto stB = [&](const frag2& X) { cplx16 c;
      c.re = MFMA32(Cc[0], X.r[0], z); c.im = MFMA32(Cc[0], X.i[0], z); c.re = MFMA32(Cc[1], X.r[1], c.re); c.im = MFMA32(Cc[1], X.i[1], c.im);
      c.re = MFMA32(Sn[0], X.i[0], c.re); c.im = MFMA32(nS[0], X.r[0], c.im); c.re = MFMA32(Sn[1], X.i[1], c.re); c.im = MFMA32(nS[1], X.r[1], c.im); return c; };
    auto cmul_pack = [&](const cplx16& c, auto&& coef) { frag2 f; u32x4 wr_[2], wi_[2];
#pragma unroll
      for (int r = 0; r < 8; r++) { f32x2 cr, ci; coef(r, cr, ci);
        const f32x2 a = {c.re[2 * r], c.re[2 * r + 1]}, b = {c.im[2 * r], c.im[2 * r + 1]};
        wr_[r >> 2][r & 3] = pk2v(a * cr - b * ci); wi_[r >> 2][r & 3] = pk2v(a * ci + b * cr); }
      f.r[0] = __builtin_bit_cast(bf16x8, wr_[0]); f.r[1] = __builtin_bit_cast(bf16x8, wr_[1]); f.i[0] = __builtin_bit_cast(bf16x8, wi_[0]); f.i[1] = __builtin_bit_cast(bf16x8, wi_[1]);
      return f; };
    auto fval = [&](const filt& f, int r, float& fr, float& fi) {
      fr = bfe(f.re[r >> 2], r & 3); fi = bfe(f.im[r >> 2], r & 3); if (r >= 8) fi = -fi;
      if (r == 0) { const float ar = 0.5f * (fr + f.x[0]), ai = 0.5f * (fi - f.x[1]); fr = lane == 0 ? ar : fr; fi = lane == 0 ? ai : fi; }
      if (r == 8) { const float ar = 0.5f * (fr + f.x[2]), ai = 0.5f * (fi + f.x[3]); fr = lane == 0 ? ar : fr; fi = lane == 0 ? ai : fi; } };
    cplx16 A = stA(X, false);
    dmaq(2);
    X = cmul_pack(A, [&](int r, f32x2& cr, f32x2& ci) { cr = twc[r]; ci = -tws[r]; });
    A = stB(X);
    dmaq(3);
    if (packed) {
      LAS char* sc = pls;
      int ln = lane; asm volatile("" : "+v"(ln)); const int l31 = ln & 31, hh = ln >> 5;
      WAITL0();
      filt fC = filter_stage(sc, rwC);
      WAITL0(); asm volatile("" ::: "memory");
      f32x16 zr;
#pragma unroll
      for (int r = 0; r < 16; r++) *(LAS float*)(sc + (32 * crow(r, hh) + l31) * 4) = A.re[r];
      WAITL0(); asm volatile("" ::: "memory");
#pragma unroll
      for (int r = 0; r < 16; r++) zr[r] = *(LAS float*)(sc + ((1024 - (32 * crow(r, hh) + l31)) & 1023) * 4);
      WAITL0(); asm volatile("" : "+v"(zr) :: "memory");
#pragma unroll
      for (int r = 0; r < 16; r++) *(LAS float*)(sc + (32 * crow(r, hh) + l31) * 4) = A.im[r];
      WAITL0(); asm volatile("" ::: "memory");
#pragma unroll
      for (int r = 0; r < 16; r++) { float ar, ai, br, bi; fval(fA, r, ar, ai); fval(fC, r, br, bi);
        const float zi = -*(LAS float*)(sc + ((1024 - (32 * crow(r, hh) + l31)) & 1023) * 4);
        const float alr = 0.5f * (ar + br), ali = 0.5f * (ai + bi), ber = 0.5f * (ar - br), bei = 0.5f * (ai - bi);
        const float a = A.re[r], b = A.im[r];
        A.re[r] = a * alr - b * ali + zr[r] * ber - zi * bei; A.im[r] = a * ali + b * alr + zr[r] * bei + zi * ber;
        if (r & 1) { asm volatile("" : "+v"(A.re[r]), "+v"(A.im[r]), "+v"(A.re[r - 1]), "+v"(A.im[r - 1]) :: "memory"); SB(); } }
      WAITL0(); asm volatile("" ::: "memory");
      X = packc(A);
    } else {
      X = cmul_pack(A, [&](int r, f32x2& cr, f32x2& ci) { float a0, b0, a1, b1; fval(fA, 2 * r, a0, b0); fval(fA, 2 * r + 1, a1, b1); cr = (f32x2){a0, a1}; ci = (f32x2){b0, b1}; });
    }
    A = stA(X, true);
    X = cmul_pack(A, [&](int r, f32x2& cr, f32x2& ci) { cr = twc[r]; ci = tws[r]; });
    A = stA(X, true);
    { int ln = lane; asm volatile("" : "+v"(ln)); const int l31 = ln & 31, hh = ln >> 5;
#pragma unroll
      for (int G = 0; G < 4; G++) { const unsigned off = (32 * l31 + 8 * G + 4 * hh) * 2;
        *(LAS u32x2*)(pls + ploff3(pA, off)) = (u32x2){pk2(A.re[4 * G], A.re[4 * G + 1]), pk2(A.re[4 * G + 2], A.re[4 * G + 3])};
        *(LAS u32x2*)(pls + ploff3(pA + 1, off)) = (u32x2){pk2(A.im[4 * G], A.im[4 * G + 1]), pk2(A.im[4 * G + 2], A.im[4 * G + 3])}; } }
  }
  WAITL0(); WST(8); BAR(); WST(9);
  STAMP(2, 3);
  { int ln = lane; asm volatile("" : "+v"(ln)); const int g = ln >> 4, q = (ln & 15) >> 2, p = ln & 3, n = ln & 15;
    const int row = sp ? ((n & 7) * 256 + (n >> 3)) : ((n >> 1) * 256 + 2 * dl + (n & 1));
    rsrc_t ro = mkrs(T2, 8 * 256 * 1024 * 2);
    s16x4 a0[4], a1[4];
#pragma unroll
    for (int t = 0; t < 4; t++) { const int d0 = wid * 128 + t * 32; a0[t] = trd(pls + ploff3(4 * g + q, (d0 + 8 * p) * 2)); a1[t] = trd(pls + ploff3(4 * g + q, (d0 + 8 * p + 4) * 2)); }
#pragma unroll
    for (int t = 0; t < 4; t++) { const int d0 = wid * 128 + t * 32;
      f32x4 d0v = __builtin_amdgcn_mfma_f32_16x16x16bf16_1k(a0[t], Mi, (f32x4){0.f, 0.f, 0.f, 0.f}, 0, 0, 0);
      f32x4 d1v = __builtin_amdgcn_mfma_f32_16x16x16bf16_1k(a1[t], Mi, (f32x4){0.f, 0.f, 0.f, 0.f}, 0, 0, 0);
      __builtin_amdgcn_raw_buffer_store_b128((u32x4){pk2(d0v[0], d0v[1]), pk2(d0v[2], d0v[3]), pk2(d1v[0], d1v[1]), pk2(d1v[2], d1v[3])}, ro, (row * 1024 + d0 + 8 * g) * 2, 0, 16); } }
  WST(10);
  STAMP(2, 4);
}

constexpr int G2_B = 16384, G2_W0 = 4 * G2_B, G2_PL = 8 * G2_B, G2_LDS = G2_PL + 32768;
#define MAP_MIXED 1
#define K0STEPS 18
constexpr int NP = K0STEPS, NM = 56 - K0STEPS, NF = NM + 4;
__global__ __launch_bounds__(512, 2) void k_gemm2(const u16* __restrict__ h, const u16* __restrict__ w2, const u16* __restrict__ b2, const u16* __restrict__ T,
                                                  const u16* __restrict__ wr, const u16* __restrict__ wi, u16* __restrict__ T2,
                                                  const float* __restrict__ g, u16* __restrict__ out, float* __restrict__ part, unsigned* __restrict__ flags) {
  extern __shared__ __attribute__((aligned(16))) char smem[];
  LAS char* lds = (LAS char*)smem;
  const int tid = threadIdx.x, lane = tid & 63, wid = __builtin_amdgcn_readfirstlane(tid >> 6);
  const int b = blockIdx.x, xcd = b & 7, idx = b >> 3;
  const int kh = idx & 1, mb = (xcd & 3) * 4 + ((idx >> 1) & 3), nb = (xcd >> 2) * 4 + (idx >> 3);
  const int pair = mb * 8 + nb;
  const int beta = mb >> 1, alpha0 = (mb & 1) * 128, d0 = nb * 128;
  const bool isP = (kh == 0);
  STAMP(3, 0);
  rsrc_t rh = mkrs((const char*)h + (size_t)mb * 8 * 112 * 1024, 8 * 112 * 1024);
  rsrc_t rw = mkrs(w2 + d0, (F_ * D_ - d0) * 2);
  const unsigned vh = lane * 16;
  unsigned vw[2];
#pragma unroll
  for (int i = 0; i < 2; i++) { const int r = lane >> 4, k = 4 * i + r; vw[i] = r * 2048 + (((lane & 15) ^ ((k & 7) << 1)) * 16); }
  if (isP) {
    spectral_unit(lds + G2_PL, lds + (wid < 4 ? 3 * G2_B + wid * 4096 : G2_W0 + 3 * G2_B + (wid - 4) * 4096), lds + 2 * G2_B, pair, wid, lane, T, wr, wi, T2, [&](int st) {
#pragma unroll
      for (int i = 0; i < 2; i++) { const int j = wid * 2 + i; dma16(rh, lds + st * G2_B + j * 1024, vh, ((j >> 1) * 112 + 2 * st + (j & 1)) * 1024); }
#pragma unroll
      for (int i = 0; i < 2; i++) { const int j = wid * 2 + i; dma16(rw, lds + G2_W0 + st * G2_B + j * 1024, vw[i & 1], (st * 64 + j * 4) * 2048); }
    });
  }
  if (wid >= 4) {
    const int lw = wid - 4;
    auto stage = [&](int kk, int buf) {
#pragma unroll
      for (int i = 0; i < 4; i++) { const int j = lw * 4 + i; dma16(rh, lds + buf * G2_B + j * 1024, vh, ((j >> 1) * 112 + 2 * kk + (j & 1)) * 1024); }
#pragma unroll
      for (int i = 0; i < 4; i++) { const int j = lw * 4 + i; dma16(rw, lds + G2_W0 + buf * G2_B + j * 1024, vw[i & 1], (kk * 64 + j * 4) * 2048); }
    };
    if (isP) {
      WAITV(16); BAR();
      for (int kt = 0; kt < NP - 1; kt++) {
        if (kt == 0) WAITV(12); else if (kt < NP - 3) WAITV(16); else if (kt == NP - 3) WAITV(8); else WAITV(0);
        BAR();
        if (kt + 4 < NP) stage(kt + 4, kt & 3);
      }
      BAR();
      return;
    }
    rsrc_t re = mkrs((const char*)&ETAB2 + (mb & 1) * 8 * 8 * 1024, 8 * 8 * 1024);
    rsrc_t rt = mkrs(T2 + beta * 256 * 1024 + d0, (256 * 1024 - d0) * 2);
    rsrc_t rp = mkrs((const char*)part + (size_t)pair * 65536, 65536);
    stage(NP + 0, 0); stage(NP + 1, 1); stage(NP + 2, 2); stage(NP + 3, 3);
    WAITV(24); BAR();
    for (int kt = 0; kt < NF - 1; kt++) {
      if (kt < NF - 3 || kt >= NM) WAITV(16); else WAITV(16);
      if (kt == NF - 2) WAITV(16);
      BAR();
      const int s = kt + 4, buf = kt & 3;
      if (s < NM) stage(NP + s, buf);
      else if (s < NF) { const int k2 = s - NM;
#pragma unroll
        for (int i = 0; i < 4; i++) { const int j = lw * 4 + i; dma16(re, lds + buf * G2_B + j * 1024, vh, ((j >> 1) * 8 + 2 * k2 + (j & 1)) * 1024); }
#pragma unroll
        for (int i = 0; i < 4; i++) { const int j = lw * 4 + i; dma16(rt, lds + G2_W0 + buf * G2_B + j * 1024, vw[i & 1], (k2 * 64 + j * 4) * 2048); }
      } else if (s < NF + 2) { const int hf = s - NF;
#pragma unroll
        for (int i = 0; i < 4; i++) { const int j = lw * 4 + i; dma16(rp, lds + buf * G2_B + j * 1024, vh, (hf * 32 + j) * 1024); }
#pragma unroll
        for (int i = 0; i < 4; i++) { const int j = lw * 4 + i; dma16(rp, lds + G2_W0 + buf * G2_B + j * 1024, vh, (hf * 32 + 16 + j) * 1024); }
      }
    }
    WAITV(0); BAR();
    return;
  }
  const int wm = wid >> 1, wn = wid & 1;
  const int gq = lane >> 4, q = (lane & 15) >> 2, p = lane & 3;
  STAMP(3, 1);
  LAS char* ha = lds + (4 * wm) * 2048 + lane * 16;
  unsigned wa[4];
  { const int kl = 4 * gq + q;
    const unsigned a0 = G2_W0 + kl * 256 + ((kl & 7) << 5) + ((p >> 1) << 4) + ((p & 1) << 3);
#pragma unroll
    for (int t = 0; t < 4; t++) wa[t] = a0 ^ ((4 * wn + t) << 5); }
  f32x4 acc[4][4];
#pragma unroll
  for (int t = 0; t < 4; t++)
#pragma unroll
    for (int i = 0; i < 4; i++) acc[t][i] = (f32x4){0.f, 0.f, 0.f, 0.f};
  bf16x8 hf[2][4];
  s16x4 wl[2][4], wh[2][4];
#define G2RDH(set, buf, ks, i) hf[set][i] = ld128(ha + (buf) * G2_B + (i) * 2048 + (ks) * 1024)
#define G2RDWL(set, buf, ks, t) wl[set][t] = trd(lds + wa[t] + (buf) * G2_B + (ks) * 8192)
#define G2RDWH(set, buf, ks, t) wh[set][t] = trd(lds + wa[t] + (buf) * G2_B + (ks) * 8192 + 4096)
#define G2RDS(ns, buf, ks, n) do { if ((n) < 4) G2RDH(ns, buf, ks, (n) & 3); else if ((n) < 12) { if (((n) & 1) == 0) G2RDWL(ns, buf, ks, (((n) - 4) >> 1) & 3); else G2RDWH(ns, buf, ks, (((n) - 4) >> 1) & 3); } } while (0)
#define G2PHASE(A, cs, ns, buf, ks, DO_RD) do { \
    _Pragma("unroll") for (int s_ = 0; s_ < 16; s_++) { A[s_ >> 2][s_ & 3] = MFMA16(cat8(wl[cs][s_ >> 2], wh[cs][s_ >> 2]), hf[cs][s_ & 3], A[s_ >> 2][s_ & 3]); if (DO_RD) G2RDS(ns, buf, ks, s_); SB(); } \
    WAITL0(); } while (0)
#define G2STEP(A, buf) do { G2PHASE(A, 0, 1, buf, 1, true); BAR(); SB(); G2PHASE(A, 1, 0, ((buf) + 1) & 3, 0, true); } while (0)
#define G2LAST(A, buf) do { G2PHASE(A, 0, 1, buf, 1, true); G2PHASE(A, 1, 0, 0, 0, false); } while (0)
  if (isP) {
    WAITV(16); BAR();
#pragma unroll
    for (int n = 0; n < 12; n++) G2RDS(0, 0, 0, n);
    WAITL0();
    G2PHASE(acc, 0, 1, 0, 1, true); WAITV(12); BAR(); SB(); G2PHASE(acc, 1, 0, 1, 0, true);
    G2PHASE(acc, 0, 1, 1, 1, true); WAITV(8); BAR(); SB(); G2PHASE(acc, 1, 0, 2, 0, true);
    WAITV(0);
    G2PHASE(acc, 0, 1, 2, 1, true); BAR(); SB();
    if (tid == 0) __hip_atomic_fetch_add((gu32*)(flags + FLAG_BDONE), 1u, __ATOMIC_RELAXED, __HIP_MEMORY_SCOPE_AGENT);
    G2PHASE(acc, 1, 0, 3, 0, true);
    G2STEP(acc, 3);
    for (int k4 = 1; k4 < (NP - 1) / 4; k4++) { G2STEP(acc, 0); G2STEP(acc, 1); G2STEP(acc, 2); G2STEP(acc, 3); }
    constexpr int R0 = ((NP - 1) / 4) * 4;
    if (R0 + 0 < NP - 1) G2STEP(acc, 0);
    if (R0 + 1 < NP - 1) G2STEP(acc, 1);
    if (R0 + 2 < NP - 1) G2STEP(acc, 2);
    G2LAST(acc, (NP - 1) & 3);
    STAMP(3, 2);
    rsrc_t rp = mkrs((const char*)part + (size_t)pair * 65536, 65536);
    const unsigned po = wid * 16384 + lane * 16;
#pragma unroll
    for (int t = 0; t < 4; t++)
#pragma unroll
      for (int i = 0; i < 4; i++) __builtin_amdgcn_raw_buffer_store_b128(__builtin_bit_cast(u32x4, acc[t][i]), rp, po + (t * 4 + i) * 1024, 0, 16);
    WAITV(0); BAR();
    if (tid == 0) __hip_atomic_store((gu32*)(flags + FLAG_PAIR + 32 * pair), 1u, __ATOMIC_RELAXED, __HIP_MEMORY_SCOPE_AGENT);
    STAMP(3, 3);
    return;
  }
  float gg[4]; u32x2 bv[4];
#pragma unroll
  for (int i = 0; i < 4; i++) { gg[i] = g[256 * beta + alpha0 + 64 * wm + 16 * i + (lane & 15)]; bv[i] = *(const u32x2*)(b2 + d0 + 64 * wn + 16 * i + 4 * gq); }
  f32x4 aca[4][4];
#pragma unroll
  for (int t = 0; t < 4; t++)
#pragma unroll
    for (int i = 0; i < 4; i++) aca[t][i] = (f32x4){0.f, 0.f, 0.f, 0.f};
  unsigned pv = 0;
  auto poll_issue = [&](unsigned* fl) { if (wid == 0) asm volatile("global_load_dword %0, %1, %2 sc1" : "=v"(pv) : "v"(0u), "s"(fl) : "memory"); };
  auto poll_check = [&](unsigned* fl, unsigned want) {
    if (wid == 0) {
      asm volatile("s_waitcnt vmcnt(0)" : "+v"(pv) :: "memory");
      unsigned spins = 0, cur = __builtin_amdgcn_readfirstlane(pv);
      while (cur < want) { if (++spins > (1u << 22)) break; __builtin_amdgcn_s_sleep(1);
        cur = __builtin_amdgcn_readfirstlane(__hip_atomic_load((gu32*)fl, __ATOMIC_RELAXED, __HIP_MEMORY_SCOPE_AGENT)); }
      __builtin_amdgcn_fence(__ATOMIC_ACQUIRE, "agent");
    } };
  auto poll_done = [&]() { if (wid == 0) WAITV(0); };
  BAR();
  STAMP(3, 2);
#pragma unroll
  for (int n = 0; n < 12; n++) G2RDS(0, 0, 0, n);
  WAITL0();
  constexpr int NG = (NM - 6) / 4;
  for (int k4 = 0; k4 < NG; k4++) { G2STEP(acc, 0); G2STEP(acc, 1); G2STEP(acc, 2); G2STEP(acc, 3); }
#define G2STEPW(A, st, AW) do { G2PHASE(A, 0, 1, (st) & 3, 1, true); AW; BAR(); SB(); G2PHASE(A, 1, 0, ((st) + 1) & 3, 0, true); } while (0)
#pragma unroll
  for (int st = NG * 4; st < NM; st++) {
    if (st == NM - 6) G2STEPW(acc, st, poll_issue(flags + FLAG_BDONE));
    else if (st == NM - 5) G2STEPW(acc, st, poll_check(flags + FLAG_BDONE, 128u));
    else if (st == NM - 4) G2STEPW(acc, st, poll_done());
    else if (st == NM - 2) G2STEPW(acc, st, poll_issue(flags + FLAG_PAIR + 32 * pair));
    else if (st == NM - 1) G2STEPW(acc, st, poll_check(flags + FLAG_PAIR + 32 * pair, 1u));
    else G2STEP(acc, st & 3);
  }
  STAMP(3, 3);
#pragma unroll
  for (int st = NM; st < NF - 1; st++) {
    if (st == NM) G2STEPW(aca, st, poll_done());
    else G2STEP(aca, st & 3);
  }
  G2LAST(aca, (NF - 1) & 3);
  STAMP(3, 4);
  BAR();
  STAMP(3, 5);
  LAS char* pb = lds + ((wid & 1) ? G2_W0 : 0) + ((NM + (wid >> 1)) & 3) * G2_B + lane * 16;
#pragma unroll
  for (int t = 0; t < 4; t++) {
    const int dd = d0 + 64 * wn + 16 * t + 4 * gq;
    const float bb[4] = {__uint_as_float(bv[t][0] << 16), __uint_as_float(bv[t][0] & 0xffff0000u), __uint_as_float(bv[t][1] << 16), __uint_as_float(bv[t][1] & 0xffff0000u)};
#pragma unroll
    for (int i = 0; i < 4; i++) {
      const int al = alpha0 + 64 * wm + 16 * i + (lane & 15);
      const f32x4 pp = *(LAS f32x4*)(pb + (t * 4 + i) * 1024);
      const float og = rbf(1.f - gg[i]);
      float o[4];
#pragma unroll
      for (int r = 0; r < 4; r++) o[r] = gg[i] * aca[t][i][r] + og * (acc[t][i][r] + pp[r] + bb[r]);
      *(u32x2*)(out + (8 * al + beta) * D_ + dd) = (u32x2){pk2(o[0], o[1]), pk2(o[2], o[3])};
    }
  }
  STAMP(3, 6);
}

extern "C" void kernel_launch(void* const* d_in, const int* in_sizes, int n_in, void* d_out, int out_size, void* d_ws, size_t ws_size, hipStream_t stream) {
  const u16* x = (const u16*)d_in[0]; const u16* wr = (const u16*)d_in[1]; const u16* wi = (const u16*)d_in[2];
  const u16* w1 = (const u16*)d_in[4]; const u16* b1 = (const u16*)d_in[5]; const u16* w2 = (const u16*)d_in[6]; const u16* b2 = (const u16*)d_in[7];
  const u16* gw = (const u16*)d_in[8];
  char* ws = (char*)d_ws;
  float* g = (float*)ws;
  unsigned* flags = (unsigned*)(ws + (64 << 10));
  u16* T = (u16*)(ws + (1 << 20));
  u16* T2 = (u16*)(ws + (5 << 20));
  u16* h = (u16*)(ws + (9 << 20));
  float* part = (float*)(ws + (24 << 20));
  (void)hipFuncSetAttribute((const void*)k_gemm1, hipFuncAttributeMaxDynamicSharedMemorySize, G1_LDS);
  (void)hipFuncSetAttribute((const void*)k_gemm2, hipFuncAttributeMaxDynamicSharedMemorySize, G2_LDS);
  k_gemm1<<<256, 512, G1_LDS, stream>>>(x, w1, b1, gw, h, T, g, flags, w2, wr, wi);
  k_gemm2<<<256, 512, G2_LDS, stream>>>(h, w2, b2, T, wr, wi, T2, g, (u16*)d_out, part, flags);
}
```

```cpp
#include <hip/hip_runtime.h>
#include <stdint.h>
typedef unsigned short u16;
#define LAS __attribute__((address_space(3)))
#define DI __device__ __forceinline__
typedef short s16x4 __attribute__((ext_vector_type(4)));
typedef short s16x8 __attribute__((ext_vector_type(8)));
typedef __bf16 bf16x8 __attribute__((ext_vector_type(8)));
typedef __bf16 bf16x2 __attribute__((ext_vector_type(2)));
typedef float f32x2 __attribute__((ext_vector_type(2)));
typedef float f32x4 __attribute__((ext_vector_type(4)));
typedef float f32x16 __attribute__((ext_vector_type(16)));
typedef unsigned u32x2 __attribute__((ext_vector_type(2)));
typedef unsigned u32x4 __attribute__((ext_vector_type(4)));
typedef __amdgpu_buffer_rsrc_t rsrc_t;

constexpr int S_ = 2048, D_ = 1024, F_ = 3584, DF_ = 513;

namespace ct {
constexpr double PI = 3.14159265358979323846;
constexpr double ccos(double x) { double x2 = x * x, t = 1, s = 1; for (int i = 1; i < 16; i++) { t *= -x2 / ((2 * i - 1) * (2 * i)); s += t; } return s; }
constexpr double cos256(int m) { m &= 255; if (m > 128) m = 256 - m; bool neg = false; if (m > 64) { m = 128 - m; neg = true; } double v = (m == 64) ? 0.0 : ccos(2 * PI * m / 256.0); return neg ? -v : v; }
constexpr u16 tobf(double d) { float f = (float)d; unsigned u = __builtin_bit_cast(unsigned, f); u += 0x7fffu + ((u >> 16) & 1u); return (u16)(u >> 16); }
struct Tab { u16 c[256]; };
constexpr Tab mktab() { Tab t{}; for (int i = 0; i < 256; i++) t.c[i] = tobf(cos256(i)); return t; }
constexpr Tab COS = mktab();
constexpr u16 E(int dp, int a) { if (dp == 0) return COS.c[0]; if (dp == 1) return COS.c[(128 * a) & 255];
  int dl = dp >> 1; int m = (dl * a) & 255; return (dp & 1) ? COS.c[(m + 64) & 255] : COS.c[m]; }
struct Chunk { u16 v[8192]; };
template <bool TR> constexpr Chunk mk(int ch) { Chunk c{}; for (int i = 0; i < 8192; i++) { int j = i & 7, lane = (i >> 3) & 63, kb = (i >> 9) & 7, blk = ch * 2 + (i >> 12);
  int col = blk * 16 + (lane & 15); int k = 32 * kb + 16 * (j >> 2) + 4 * (lane >> 4) + (j & 3); c.v[i] = TR ? E(k, col) : E(col, k); } return c; }
template <bool TR, int I> constexpr Chunk CH = mk<TR>(I);
constexpr Chunk mk32(int blk) { Chunk c{}; for (int i = 0; i < 8192; i++) { int j = i & 7, lane = (i >> 3) & 63, kb = i >> 9;
  c.v[i] = E(16 * kb + 8 * (lane >> 5) + j, 32 * blk + (lane & 31)); } return c; }
template <int I> constexpr Chunk CH32 = mk32(I);
struct Big { Chunk c[8]; };
}
__device__ __attribute__((aligned(1024))) const ct::Big ETAB1 = {{ct::CH<false,0>, ct::CH<false,1>, ct::CH<false,2>, ct::CH<false,3>, ct::CH<false,4>, ct::CH<false,5>, ct::CH<false,6>, ct::CH<false,7>}};
__device__ __attribute__((aligned(1024))) const ct::Big ETAB2 = {{ct::CH32<0>, ct::CH32<1>, ct::CH32<2>, ct::CH32<3>, ct::CH32<4>, ct::CH32<5>, ct::CH32<6>, ct::CH32<7>}};

namespace ct {
constexpr double cosq(int m, int n) {
  m %= n; if (m < 0) m += n; if (2 * m > n) m = n - m; bool neg = false; if (4 * m > n) { m = n / 2 - m; neg = true; }
  double v = (4 * m == n) ? 0.0 : ((8 * m > n) ? 0.0 : 0.0);
  if (4 * m != n) { double x = 2 * PI * m / n; v = ccos(x); }
  return neg ? -v : v; }
struct C2048 { double c[513]; };
constexpr C2048 mk2048() { C2048 t{}; for (int i = 0; i <= 512; i++) t.c[i] = (i == 512) ? 0.0 : ccos(2 * PI * i / 2048.0); return t; }
constexpr C2048 CQ = mk2048();
constexpr double c2048(int m) { m &= 2047; if (m > 1024) m = 2048 - m; return m > 512 ? -CQ.c[1024 - m] : CQ.c[m]; }
constexpr double s2048(int m) { return c2048(m - 512); }
constexpr unsigned f2u(double d) { return __builtin_bit_cast(unsigned, (float)d); }
constexpr int crow_(int r, int h) { return (r & 3) + 8 * (r >> 2) + 4 * h; }
struct CTab { unsigned v[12 * 64 * 4]; };
constexpr CTab mkctab() { CTab t{};
  for (int lane = 0; lane < 64; lane++) { const int l31 = lane & 31, hh = lane >> 5;
    for (int s = 0; s < 2; s++) for (int e = 0; e < 4; e++) { const int k0 = crow_(8 * s + 2 * e, hh), k1 = crow_(8 * s + 2 * e + 1, hh);
      t.v[((0 + s) * 64 + lane) * 4 + e] = (unsigned)tobf(c2048(64 * l31 * k0)) | ((unsigned)tobf(c2048(64 * l31 * k1)) << 16);
      t.v[((2 + s) * 64 + lane) * 4 + e] = (unsigned)tobf(s2048(64 * l31 * k0)) | ((unsigned)tobf(s2048(64 * l31 * k1)) << 16); }
    for (int r = 0; r < 16; r++) { t.v[((4 + (r >> 2)) * 64 + lane) * 4 + (r & 3)] = f2u(c2048(2 * crow_(r, hh) * l31)); t.v[((8 + (r >> 2)) * 64 + lane) * 4 + (r & 3)] = f2u(s2048(2 * crow_(r, hh) * l31)); } }
  return t; }
struct MChunk { unsigned v[8 * 64 * 4]; };
constexpr int jp_(bool sp, int dl, int m) { return sp ? (m < 2 ? 1024 * m : (m < 8 ? 256 * (m >> 1) : 128 + 256 * ((m - 8) >> 1))) : dl + 256 * (m >> 1); }
constexpr MChunk mkm(int ch) { MChunk t{};
  for (int u = 0; u < 8; u++) for (int lane = 0; lane < 64; lane++) { const int dl = ch * 8 + u; const bool sp = dl == 0; const int n = lane & 15, g = lane >> 4; u16 f[4] = {}, iv[4] = {};
    for (int j = 0; j < 4; j++) { const int k = 4 * g + j; const bool ok = !sp || ((n < 8) == (k < 8));
      { const int a = (sp ? (k & 7) : (k >> 1)) * jp_(sp, dl, n); const bool ii = !sp && (k & 1), io = (n & 1) && (!sp || n >= 2);
        const double v = (ii == io) ? c2048(a) : (ii ? s2048(a) : -s2048(a)); f[j] = tobf(ok ? v : 0.0); }
      { const int a = (sp ? (n & 7) : (n >> 1)) * jp_(sp, dl, k); const bool ii = (k & 1) && (!sp || k >= 2), io = !sp && (n & 1);
        const double v = (ii == io) ? c2048(a) : (ii ? -s2048(a) : s2048(a)); iv[j] = tobf(ok ? v * ((sp && k < 2 ? 1.0 : 2.0) / 2097152.0) : 0.0); } }
    unsigned* o = &t.v[(u * 64 + lane) * 4]; o[0] = f[0] | ((unsigned)f[1] << 16); o[1] = f[2] | ((unsigned)f[3] << 16); o[2] = iv[0] | ((unsigned)iv[1] << 16); o[3] = iv[2] | ((unsigned)iv[3] << 16); }
  return t; }
template <int I> constexpr MChunk MCH = mkm(I);
struct MBig { MChunk c[16]; };
}
__device__ __attribute__((aligned(1024))) const ct::CTab CTAB = ct::mkctab();
__device__ __attribute__((aligned(1024))) const ct::MBig MTAB = {{ct::MCH<0>, ct::MCH<1>, ct::MCH<2>, ct::MCH<3>, ct::MCH<4>, ct::MCH<5>, ct::MCH<6>, ct::MCH<7>, ct::MCH<8>, ct::MCH<9>, ct::MCH<10>, ct::MCH<11>, ct::MCH<12>, ct::MCH<13>, ct::MCH<14>, ct::MCH<15>}};

DI float bf2f(u16 v) { return __uint_as_float(((unsigned)v) << 16); }
DI unsigned pk2(float lo, float hi) { f32x2 v = {lo, hi}; bf16x2 b = __builtin_convertvector(v, bf16x2); return __builtin_bit_cast(unsigned, b); }
DI float rbf(float f) { return __uint_as_float(pk2(f, 0.f) << 16); }
DI rsrc_t mkrs(const void* p, unsigned bytes) { return __builtin_amdgcn_make_buffer_rsrc((void*)p, 0, bytes, 0x00020000); }
DI void dma16(rsrc_t rs, LAS char* dst, unsigned voff, unsigned soff) {
  asm volatile("s_mov_b32 m0, %0\n\ts_nop 0\n\tbuffer_load_dwordx4 %1, %2, %3 offen lds" :: "s"((unsigned)(size_t)dst), "v"(voff), "s"(rs), "s"(soff) : "memory");
}
DI void dma16nt(rsrc_t rs, LAS char* dst, unsigned voff, unsigned soff) {
  asm volatile("s_mov_b32 m0, %0\n\ts_nop 0\n\tbuffer_load_dwordx4 %1, %2, %3 offen nt lds" :: "s"((unsigned)(size_t)dst), "v"(voff), "s"(rs), "s"(soff) : "memory");
}
DI u32x4 gld16(const void* base, unsigned voff) { u32x4 r; asm volatile("global_load_dwordx4 %0, %1, %2" : "=v"(r) : "v"(voff), "s"(base) : "memory"); return r; }
DI void dma4(rsrc_t rs, LAS char* dst, unsigned v0, unsigned v1, unsigned v2, unsigned v3, unsigned soff) {
  asm volatile("s_mov_b32 m0, %0\n\ts_nop 0\n\tbuffer_load_dwordx4 %1, %5, %6 offen lds\n\tbuffer_load_dwordx4 %2, %5, %6 offen offset:1024 lds\n\t"
               "buffer_load_dwordx4 %3, %5, %6 offen offset:2048 lds\n\tbuffer_load_dwordx4 %4, %5, %6 offen offset:3072 lds"
               :: "s"((unsigned)(size_t)dst), "v"(v0), "v"(v1), "v"(v2), "v"(v3), "s"(rs), "s"(soff) : "memory");
}
DI s16x4 trd(LAS char* p) { return __builtin_amdgcn_ds_read_tr16_b64_v4i16((LAS s16x4*)p); }
DI bf16x8 cat8(s16x4 a, s16x4 b) { return __builtin_bit_cast(bf16x8, __builtin_shufflevector(a, b, 0, 1, 2, 3, 4, 5, 6, 7)); }
DI bf16x8 ld128(LAS char* p) { return *(LAS bf16x8*)p; }
#define MFMA16(a, b, c) __builtin_amdgcn_mfma_f32_16x16x32_bf16((a), (b), (c), 0, 0, 0)
#define MFMA32(a, b, c) __builtin_amdgcn_mfma_f32_32x32x16_bf16((a), (b), (c), 0, 0, 0)
#define WAITV(n) asm volatile("s_waitcnt vmcnt(" #n ")" ::: "memory")
#define WAITVN(n) asm volatile("s_waitcnt vmcnt(%0)" :: "n"(n) : "memory")
#define WAITL(n) asm volatile("s_waitcnt lgkmcnt(" #n ")" ::: "memory")
#define WAITL0() __builtin_amdgcn_s_waitcnt(0xC07F)
#define BAR() __builtin_amdgcn_s_barrier()

DI unsigned pk2v(f32x2 v) { bf16x2 b = __builtin_convertvector(v, bf16x2); return __builtin_bit_cast(unsigned, b); }
DI unsigned gelu2(float a, float b) {
  const float c1 = -2.302208198f, c2 = -0.1029432397f;
  f32x2 v = {a, b};
  f32x2 t = v * v; f32x2 p = t * c2 + c1; f32x2 z = v * p;
  f32x2 e; e.x = __builtin_amdgcn_exp2f(z.x); e.y = __builtin_amdgcn_exp2f(z.y);
  f32x2 d = e + 1.f; f32x2 r; r.x = __builtin_amdgcn_rcpf(d.x); r.y = __builtin_amdgcn_rcpf(d.y);
  return pk2v(v * r);
}

#define SB() __builtin_amdgcn_sched_barrier(0)
#define HAUX 16
#define K0STEPS 20
#define PFS 4
constexpr int NP_ = K0STEPS;
#define K1_BYTES 14336
#define K2_BYTES 25600
#define OUT_AUX 0
typedef __attribute__((address_space(1))) unsigned gu32;
constexpr int FLAG_BDONE = 0;
constexpr int FLAG_PAIR = 32;

constexpr int G1_XB = 16384, G1_WB = 32768, G1_W0 = 3 * G1_XB, G1_BI = G1_W0 + 3 * G1_WB, G1_GW = G1_BI + 1024, G1_CNT = G1_GW + 2048, G1_LDS = G1_CNT + 16;
DI const char* code_k1() { unsigned l, h; asm volatile("s_getpc_b64 s[90:91]\n\ts_add_u32 s90, s90, k_gemm1@rel32@lo+4\n\ts_addc_u32 s91, s91, k_gemm1@rel32@hi+12\n\ts_mov_b32 %0, s90\n\ts_mov_b32 %1, s91" : "=s"(l), "=s"(h) :: "scc", "s90", "s91");
  return (const char*)(((unsigned long long)h << 32) | l); }
DI const char* code_k2() { unsigned l, h; asm volatile("s_getpc_b64 s[90:91]\n\ts_add_u32 s90, s90, k_gemm2@rel32@lo+4\n\ts_addc_u32 s91, s91, k_gemm2@rel32@hi+12\n\ts_mov_b32 %0, s90\n\ts_mov_b32 %1, s91" : "=s"(l), "=s"(h) :: "scc", "s90", "s91");
  return (const char*)(((unsigned long long)h << 32) | l); }
DI void touch255(const void* p) { asm volatile("global_load_dword v255, %0, off" :: "v"(p) : "v255", "memory"); }
extern "C" __global__ __launch_bounds__(512, 2) void k_gemm1(const u16* __restrict__ x, const u16* __restrict__ w1, const u16* __restrict__ b1, const u16* __restrict__ gw,
                                                  u16* __restrict__ h, u16* __restrict__ T, float* __restrict__ g, unsigned* __restrict__ flags,
                                                  const u16* __restrict__ w2, const u16* __restrict__ wr, const u16* __restrict__ wi) {
  extern __shared__ __attribute__((aligned(16))) char smem[];
  LAS char* lds = (LAS char*)smem;
  const int tid = threadIdx.x, lane = tid & 63, wid = __builtin_amdgcn_readfirstlane(tid >> 6);
  const int b = blockIdx.x, xcd = b & 7, idx = b >> 3;
  const int mb = (xcd & 1) * 8 + (idx & 7), nb = (xcd >> 1) * 4 + (idx >> 3);
  if (wid == 0) { const unsigned e = (idx * 8 + (lane & 7)) * 64; touch255(code_k1() + (e < K1_BYTES ? e : 0)); }
  if (wid >= 4) {
    const int lw = wid - 4;
    const int beta2 = b & 7, dblk = (b >> 3) & 15, dh = b >> 7;
    LAS char* tl = lds + G1_W0 + G1_WB;
    u32x4 ef[2][8];
    {
      const int beta = mb >> 1, alpha0 = (mb & 1) * 128;
      const unsigned xo = (8 * alpha0 + beta) * D_;
      const unsigned wo = (nb * 448) & ~127u;
      rsrc_t rx = mkrs(x + xo, (S_ * D_ - xo) * 2);
      rsrc_t rw = mkrs((const char*)w1 + wo, D_ * F_ * 2 - wo);
      rsrc_t rb = mkrs(b1 + nb * 224, 448);
      unsigned vx[4], vw[8];
      { const int r = lane >> 3, c1 = lane & 7;
#pragma unroll
        for (int i = 0; i < 4; i++) vx[i] = r * 16384 + ((c1 ^ ((i & 1) * 4 + (r >> 1))) * 16) + i * (131072 - 1024); }
#pragma unroll
      for (int i = 0; i < 8; i++) { const int hi = lane >> 5, k = 2 * i + hi, f = (k & 3) | (((k >> 3) & 1) << 2);
        vw[i] = hi * (F_ * 2) + (((lane & 31) ^ (f << 1)) * 16) + (i & 3) * (2 * F_ * 2 - 1024); }
      const unsigned sx0 = lw * 4 * 131072, sw0 = lw * 16 * (F_ * 2);
      auto stage = [&](int kt, int buf) {
        dma4(rx, lds + buf * G1_XB + lw * 4096, vx[0], vx[1], vx[2], vx[3], sx0 + kt * 128);
        dma4(rw, lds + G1_W0 + buf * G1_WB + lw * 8192, vw[0], vw[1], vw[2], vw[3], sw0 + kt * 64 * (F_ * 2));
        dma4(rw, lds + G1_W0 + buf * G1_WB + lw * 8192 + 4096, vw[4], vw[5], vw[6], vw[7], sw0 + (kt * 64 + 8) * (F_ * 2));
      };
      if (lw == 0) dma16(rb, lds + G1_BI, lane * 16, 0);
      if (lw == 3) { rsrc_t rg = mkrs(gw, 2048); dma16(rg, lds + G1_GW, lane * 16, 0); dma16(rg, lds + G1_GW + 1024, lane * 16, 1024); }
      stage(0, 0); stage(1, 1);
      if (lw == 1) {
        if (lane < 2) *(LAS unsigned*)(lds + G1_CNT + 4 * lane) = 0u;
        if (b < 129 && lane == 0) flags[b * 32] = 0u;
      }
      WAITV(12); WAITL(0); BAR();
      stage(2, 2);
      int cb = 0;
      const int gwv = b * 4 + lw;
      for (int kt = 0; kt < 13; kt++) {
        WAITV(12);
        BAR();
        stage(kt + 3, cb);
        cb = (cb == 2) ? 0 : cb + 1;
      }
      WAITV(12); BAR();
      {
        rsrc_t rx2 = mkrs(x + beta2 * D_ + dblk * 64, (unsigned)(S_ * D_ * 2 - (beta2 * D_ + dblk * 64) * 2));
        const int hi = lane >> 5, r = (lane & 31) >> 2, c = (lane & 3) ^ (2 * ((r >> 2) & 1));
        const unsigned voff = r * 16384 + hi * 64 + c * 16;
#pragma unroll
        for (int i = 0; i < 8; i++) { const int j = lw * 8 + i; dma16(rx2, tl + j * 1024, voff, j * 8 * 16384); }
        { rsrc_t rx3 = mkrs(x + b * 8 * D_, 8 * D_ * 2);
#pragma unroll
          for (int i = 0; i < 4; i++) dma16(rx3, lds + G1_XB + (lw * 4 + i) * 1024, lane * 16, (lw * 4 + i) * 1024); }
      }
      WAITV(12); BAR();
#pragma unroll
      for (int c2 = 0; c2 < 2; c2++)
#pragma unroll
        for (int kb = 0; kb < 8; kb++) ef[c2][kb] = gld16(&ETAB1, ((dh * 8 + lw * 2 + c2) * 8 + kb) * 1024 + lane * 16);
      touch255((const char*)w2 + (size_t)(NP_ * 64 * 16 + gwv * PFS + (lane < PFS ? lane : 0)) * 128);
      { const int l = lane & 31;
        const char* p2 = (const char*)&CTAB + (gwv % 96) * 128;
        if (l == 17) p2 = (const char*)&ETAB2 + gwv * 128;
        if (l == 18) p2 = (const char*)&MTAB + gwv * 128;
        touch255(p2); }
      { const unsigned e = ((idx * 4 + lw) * 4 + (lane & 3)) * 64; touch255(code_k2() + (e < K2_BYTES ? e : 0)); }
    }
    WAITV(19);
    if (lane == 0) __hip_atomic_fetch_add((LAS unsigned*)(lds + G1_CNT + 4), 1u, __ATOMIC_RELAXED, __HIP_MEMORY_SCOPE_WORKGROUP);
    asm volatile("s_waitcnt vmcnt(3)" : "+v"(ef[0][0]), "+v"(ef[0][1]), "+v"(ef[0][2]), "+v"(ef[0][3]), "+v"(ef[0][4]), "+v"(ef[0][5]), "+v"(ef[0][6]), "+v"(ef[0][7]),
                 "+v"(ef[1][0]), "+v"(ef[1][1]), "+v"(ef[1][2]), "+v"(ef[1][3]), "+v"(ef[1][4]), "+v"(ef[1][5]), "+v"(ef[1][6]), "+v"(ef[1][7]) :: "memory");
    if (lane == 0) __hip_atomic_fetch_add((LAS unsigned*)(lds + G1_CNT), 1u, __ATOMIC_RELAXED, __HIP_MEMORY_SCOPE_WORKGROUP);
    while (__builtin_amdgcn_readfirstlane(*(volatile LAS unsigned*)(lds + G1_CNT)) < 4u) __builtin_amdgcn_s_sleep(1);
    asm volatile("" ::: "memory");
    const int gq = lane >> 4, q = (lane & 15) >> 2, p = lane & 3;
    f32x4 acc[2][4];
#pragma unroll
    for (int c = 0; c < 2; c++)
#pragma unroll
      for (int i = 0; i < 4; i++) acc[c][i] = (f32x4){0.f, 0.f, 0.f, 0.f};
    LAS char* base = tl + (gq >> 1) * 2 * 512 + (4 * (gq & 1) + q) * 64 + ((p ^ (2 * (gq & 1))) * 16);
#pragma unroll
    for (int kb = 0; kb < 8; kb++) {
#pragma unroll
      for (int i = 0; i < 4; i++) {
        LAS char* a = base + (4 * kb) * 1024 + (i >> 1) * 512 + (i & 1) * 8;
        bf16x8 A = cat8(trd(a), trd(a + 2 * 1024));
#pragma unroll
        for (int c = 0; c < 2; c++) acc[c][i] = MFMA16(A, __builtin_bit_cast(bf16x8, ef[c][kb]), acc[c][i]);
      }
    }
    rsrc_t rT = mkrs(T, 8 * 256 * 1024 * 2);
#pragma unroll
    for (int c = 0; c < 2; c++) {
      const int dp = dh * 128 + lw * 32 + c * 16 + (lane & 15);
#pragma unroll
      for (int ip = 0; ip < 2; ip++) {
        u32x4 w;
        w[0] = pk2(acc[c][2 * ip][0], acc[c][2 * ip][1]); w[1] = pk2(acc[c][2 * ip][2], acc[c][2 * ip][3]);
        w[2] = pk2(acc[c][2 * ip + 1][0], acc[c][2 * ip + 1][1]); w[3] = pk2(acc[c][2 * ip + 1][2], acc[c][2 * ip + 1][3]);
        __builtin_amdgcn_raw_buffer_store_b128(w, rT, ((beta2 * 256 + dp) * D_ + dblk * 64 + ip * 32 + gq * 8) * 2, 0, HAUX);
      }
    }
    return;
  }
  const int wm = wid >> 1, wn = wid & 1;
  const int c = lane & 15, gq = lane >> 4, q = (lane & 15) >> 2, p = lane & 3;
  LAS char* xa0 = lds + (64 * wm + c) * 128 + ((gq ^ (c >> 1)) * 16);
  LAS char* xa1 = lds + (((64 * wm + c) * 128 + ((gq ^ (c >> 1)) * 16)) ^ 64);
  const unsigned wa0 = G1_W0 + (8 * gq + q) * 512 + ((q | ((gq & 1) << 2)) << 5) + ((p >> 1) << 4) + ((p & 1) << 3);
  const unsigned tsh = (7 * wn + 2 * (nb & 1));
  unsigned wa[7];
#pragma unroll
  for (int t = 0; t < 7; t++) wa[t] = wa0 ^ ((tsh + t) << 5);
  f32x4 acc[7][4];
  bf16x8 xf[2][4];
  s16x4 wl[2][7], wh[2][7];
  BAR();
#pragma unroll
  for (int t = 0; t < 7; t++) { u32x2 bv = *(LAS u32x2*)(lds + G1_BI + (16 * (7 * wn + t) + 4 * gq) * 2);
    f32x4 bb = {__uint_as_float(bv[0] << 16), __uint_as_float(bv[0] & 0xffff0000u), __uint_as_float(bv[1] << 16), __uint_as_float(bv[1] & 0xffff0000u)};
#pragma unroll
    for (int i = 0; i < 4; i++) acc[t][i] = bb; }
#define RDX(set, buf, ks, i) xf[set][i] = ld128(((ks) ? xa1 : xa0) + (buf) * G1_XB + (i) * 2048)
#define RDWL(set, buf, ks, t) wl[set][t] = trd(lds + wa[t] + ((buf) & 1) * G1_WB + (ks) * 16384)
#define RDWH(set, buf, ks, t) wh[set][t] = trd(lds + wa[t] + ((buf) & 1) * G1_WB + (ks) * 16384 + 2048)
#define MM(set, t, i) acc[t][i] = MFMA16(cat8(wl[set][t], wh[set][t]), xf[set][i], acc[t][i])
#define RDS(ns, buf, ks, n) do { if ((n) < 4) RDX(ns, buf, ks, (n) & 3); else if ((n) < 18) { if (((n) & 1) == 0) RDWL(ns, buf, ks, (((n) - 4) >> 1) % 7); else RDWH(ns, buf, ks, (((n) - 4) >> 1) % 7); } } while (0)
#define PHASE(cs, ns, buf, ks, DO_RD, ADJ) do { \
    _Pragma("unroll") for (int s_ = 0; s_ < 28; s_++) { MM(cs, s_ >> 2, s_ & 3); if (DO_RD) RDS(ns, buf, ks, s_); \
      if ((ADJ) != 0 && s_ >= 18 && s_ < 25) { wa[(s_ - 18) % 7] += (ADJ); asm volatile("" : "+v"(wa[(s_ - 18) % 7])); } SB(); } \
    WAITL0(); } while (0)
#pragma unroll
  for (int n = 0; n < 18; n++) RDS(0, 0, 0, n);
  WAITL0();
  for (int k3 = 0; k3 < 5; k3++) {
    PHASE(0, 1, 0, 1, true, 0); BAR(); SB(); PHASE(1, 0, 1, 0, true, 0);
    PHASE(0, 1, 1, 1, true, 2 * G1_WB); BAR(); SB(); PHASE(1, 0, 2, 0, true, 0);
    PHASE(0, 1, 2, 1, true, -2 * G1_WB); BAR(); SB(); PHASE(1, 0, 0, 0, true, 0);
  }
  PHASE(0, 1, 0, 1, true, 0);
  PHASE(1, 0, 0, 0, false, 0);
  {
    rsrc_t rhs = mkrs(h, 64 * 224 * 1024);
#pragma unroll
    for (int ip = 0; ip < 2; ip++) {
      const unsigned ho = ((mb * 4 + wm * 2 + ip) * 224 + 14 * nb + 7 * wn) * 1024 + lane * 16;
#pragma unroll
      for (int t = 0; t < 7; t++) {
        const unsigned a0 = gelu2(acc[t][2 * ip][0], acc[t][2 * ip][1]), b0 = gelu2(acc[t][2 * ip][2], acc[t][2 * ip][3]);
        const unsigned a1 = gelu2(acc[t][2 * ip + 1][0], acc[t][2 * ip + 1][1]), b1_ = gelu2(acc[t][2 * ip + 1][2], acc[t][2 * ip + 1][3]);
        const auto sa = __builtin_amdgcn_permlane16_swap(a0, a1, false, false);
        const auto sb = __builtin_amdgcn_permlane16_swap(b0, b1_, false, false);
        __builtin_amdgcn_raw_buffer_store_b128((u32x4){sa[0], sb[0], sa[1], sb[1]}, rhs, ho + t * 1024, 0, HAUX);
      }
    }
  }
  {
    while (__builtin_amdgcn_readfirstlane(*(volatile LAS unsigned*)(lds + G1_CNT + 4)) < 4u) __builtin_amdgcn_s_sleep(1);
    asm volatile("" ::: "memory");
    const bf16x8 g0 = ld128(lds + G1_GW + lane * 32), g1 = ld128(lds + G1_GW + lane * 32 + 16);
    bf16x8 x0[2], x1[2];
#pragma unroll
    for (int rr = 0; rr < 2; rr++) { x0[rr] = ld128(lds + G1_XB + (wid * 2 + rr) * 2048 + lane * 32); x1[rr] = ld128(lds + G1_XB + (wid * 2 + rr) * 2048 + lane * 32 + 16); }
    float a2[2];
#pragma unroll
    for (int rr = 0; rr < 2; rr++) { float a = 0.f;
      a = __builtin_amdgcn_fdot2_f32_bf16(__builtin_shufflevector(x0[rr], x0[rr], 0, 1), __builtin_shufflevector(g0, g0, 0, 1), a, false);
      a = __builtin_amdgcn_fdot2_f32_bf16(__builtin_shufflevector(x0[rr], x0[rr], 2, 3), __builtin_shufflevector(g0, g0, 2, 3), a, false);
      a = __builtin_amdgcn_fdot2_f32_bf16(__builtin_shufflevector(x0[rr], x0[rr], 4, 5), __builtin_shufflevector(g0, g0, 4, 5), a, false);
      a = __builtin_amdgcn_fdot2_f32_bf16(__builtin_shufflevector(x0[rr], x0[rr], 6, 7), __builtin_shufflevector(g0, g0, 6, 7), a, false);
      a = __builtin_amdgcn_fdot2_f32_bf16(__builtin_shufflevector(x1[rr], x1[rr], 0, 1), __builtin_shufflevector(g1, g1, 0, 1), a, false);
      a = __builtin_amdgcn_fdot2_f32_bf16(__builtin_shufflevector(x1[rr], x1[rr], 2, 3), __builtin_shufflevector(g1, g1, 2, 3), a, false);
      a = __builtin_amdgcn_fdot2_f32_bf16(__builtin_shufflevector(x1[rr], x1[rr], 4, 5), __builtin_shufflevector(g1, g1, 4, 5), a, false);
      a = __builtin_amdgcn_fdot2_f32_bf16(__builtin_shufflevector(x1[rr], x1[rr], 6, 7), __builtin_shufflevector(g1, g1, 6, 7), a, false);
      a2[rr] = a; }
    const auto sw = __builtin_amdgcn_permlane32_swap(__float_as_uint(a2[0]), __float_as_uint(a2[1]), false, false);
    float a = __uint_as_float(sw[0]) + __uint_as_float(sw[1]);
    a += __uint_as_float(__builtin_amdgcn_update_dpp(0u, __float_as_uint(a), 0xB1, 0xF, 0xF, false));
    a += __uint_as_float(__builtin_amdgcn_update_dpp(0u, __float_as_uint(a), 0x4E, 0xF, 0xF, false));
    a += __uint_as_float(__builtin_amdgcn_update_dpp(0u, __float_as_uint(a), 0x141, 0xF, 0xF, false));
    a += __uint_as_float(__builtin_amdgcn_update_dpp(0u, __float_as_uint(a), 0x140, 0xF, 0xF, false));
    { const auto s16 = __builtin_amdgcn_permlane16_swap(__float_as_uint(a), __float_as_uint(a), false, false); a = __uint_as_float(s16[0]) + __uint_as_float(s16[1]); }
    if ((lane & 31) == 0) { const int s = b * 8 + wid * 2 + (lane >> 5); const float z = rbf(a); g[256 * (s & 7) + (s >> 3)] = rbf(1.f / (1.f + __expf(-z))); }
  }
}

DI int crow(int r, int h) { return (r & 3) + 8 * (r >> 2) + 4 * h; }
DI bf16x8 pack8(const f32x16& v, int s) { u32x4 w; w[0] = pk2(v[8 * s], v[8 * s + 1]); w[1] = pk2(v[8 * s + 2], v[8 * s + 3]); w[2] = pk2(v[8 * s + 4], v[8 * s + 5]); w[3] = pk2(v[8 * s + 6], v[8 * s + 7]); return __builtin_bit_cast(bf16x8, w); }
DI unsigned ploff(int pl, unsigned bb) { return pl * 2048 + (bb ^ ((pl & 7) << 5)); }
DI unsigned ploff2(int pl, unsigned bb) { return pl * 2048 + (bb ^ (pl << 3)); }
DI unsigned ploff3(int pl, unsigned bb) { return pl * 2048 + (bb ^ (((bb >> 7) & 7) << 3) ^ ((pl & 1) << 3) ^ (((pl >> 1) & 3) << 6)); }
DI float bfe(s16x4 v, int e) { return __uint_as_float(((unsigned)(u16)v[e]) << 16); }
struct cplx16 { f32x16 re, im; };
struct frag2 { bf16x8 r[2], i[2]; };
DI frag2 packc(const cplx16& c) { frag2 f; f.r[0] = pack8(c.re, 0); f.r[1] = pack8(c.re, 1); f.i[0] = pack8(c.im, 0); f.i[1] = pack8(c.im, 1); return f; }
#define SGB(mask, n) __builtin_amdgcn_sched_group_barrier(mask, n, 0)
#define INTERLEAVE(NV) do { SGB(0x8, 1); SGB(0x2, NV); SGB(0x8, 1); SGB(0x2, NV); SGB(0x8, 1); SGB(0x2, NV); SGB(0x8, 1); SGB(0x2, NV); \
                            SGB(0x8, 1); SGB(0x2, NV); SGB(0x8, 1); SGB(0x2, NV); SGB(0x8, 1); SGB(0x2, NV); SGB(0x8, 1); SGB(0x2, NV); SB(); } while (0)
constexpr int SP_FX = 13312, SP_CNT = 13824;
DI int unit_j(int dl, int r) { return dl == 0 ? (r < 4 ? 256 * r : 128 + 256 * (r - 4)) : dl + 256 * r; }
template <class F>
DI void spectral_unit(LAS char* pls, LAS char* fsa, LAS char* fsb, LAS char* fsx, LAS char* cst, const int dl, const int wid, const int lane,
                      const u16* __restrict__ T, const u16* __restrict__ wr, const u16* __restrict__ wi, u16* __restrict__ T2, F&& dmaq) {
  const bool sp = (dl == 0);
  {
    rsrc_t rt = mkrs(T, 8 * 256 * 1024 * 2);
    const int p0 = 2 * wid, p1 = p0 + 1;
    const int r0 = sp ? ((p0 & 7) * 256 + (p0 >> 3)) : (wid * 256 + 2 * dl), r1 = sp ? ((p1 & 7) * 256 + (p1 >> 3)) : r0 + 1;
    const unsigned va = (lane ^ ((p0 & 7) << 1)) * 16, vb = (lane ^ ((p1 & 7) << 1)) * 16 + (r1 - r0 - 1) * 2048;
    dma4(rt, pls + p0 * 2048, va, va, vb, vb, r0 * 2048);
    if (wid < 3) { rsrc_t rc = mkrs(&CTAB, 12 * 1024); dma4(rc, cst + wid * 4096, lane * 16, lane * 16, lane * 16, lane * 16, wid * 4096); }
    else if (wid == 3) { rsrc_t rm = mkrs((const char*)&MTAB + dl * 1024, 1024); dma16(rm, cst + 12 * 1024, lane * 16, 0);
      if (lane == 0) *(LAS unsigned*)(cst + SP_CNT) = 0u; }
    else {
      const int lw = wid - 4;
      rsrc_t rr = mkrs(wr, S_ * DF_ * 2), ri = mkrs(wi, S_ * DF_ * 2);
#pragma unroll
      for (int k = 0; k < 2; k++) { const int r = 2 * lw + k, j = unit_j(dl, r), jm = (2048 - j) & 2047;
        LAS char* fs = (r < 4 ? fsa : fsb) + (r & 3) * 4096;
        dma16nt(rr, fs, lane * 16, j * DF_ * 2); dma16nt(rr, fs + 1024, lane * 16, (jm * DF_ + 1) * 2);
        dma16nt(ri, fs + 2048, lane * 16, j * DF_ * 2); dma16nt(ri, fs + 3072, lane * 16, (jm * DF_ + 1) * 2);
      }
      dma16((lw & 2) ? ri : rr, fsx + lw * 1024, lane * 16, (1024 * DF_ + (lw & 1)) * 2);
      { const int r = (lane & 15) >> 1, j = unit_j(dl, r), jm = (2048 - j) & 2047; const unsigned vo = ((lane & 1) ? j * DF_ + 512 : jm * DF_) * 2;
        asm volatile("s_mov_b32 m0, %0\n\ts_nop 0\n\tbuffer_load_ushort %1, %2, 0 offen lds" :: "s"((unsigned)(size_t)(cst + SP_FX + (lw & 1) * 256)), "v"(vo), "s"((lw & 1) ? ri : rr) : "memory"); }
    }
  }
  asm volatile("s_cmp_lt_u32 %0, 4\n\ts_cbranch_scc1 1f\n\ts_waitcnt vmcnt(10)\n\ts_branch 2f\n1:\n\ts_waitcnt vmcnt(0)\n2:\n\ts_waitcnt lgkmcnt(0)" :: "s"(wid) : "memory", "scc");
  BAR();
  if (wid >= 4) dmaq(-1);
  const u32x4 mt_ = *(LAS u32x4*)(cst + 12 * 1024 + lane * 16);
  const s16x4 Mf = __builtin_bit_cast(s16x4, (u32x2){mt_[0], mt_[1]}), Mi = __builtin_bit_cast(s16x4, (u32x2){mt_[2], mt_[3]});
  { int ln = lane; asm volatile("" : "+v"(ln)); const int g = ln >> 4, q = (ln & 15) >> 2, p = ln & 3, n = ln & 15;
    s16x4 a[8]; f32x4 dd[8];
#pragma unroll
    for (int t = 0; t < 8; t++) a[t] = trd(pls + ploff(4 * g + q, (wid * 128 + t * 16 + 4 * p) * 2));
#pragma unroll
    for (int t = 0; t < 8; t++) dd[t] = __builtin_amdgcn_mfma_f32_16x16x16bf16_1k(a[t], Mf, (f32x4){0.f, 0.f, 0.f, 0.f}, 0, 0, 0);
    SB();
#pragma unroll
    for (int t = 0; t < 8; t++) *(LAS u32x2*)(pls + ploff2(n, (wid * 128 + t * 16 + 4 * g) * 2)) = (u32x2){pk2(dd[t][0], dd[t][1]), pk2(dd[t][2], dd[t][3])}; }
  WAITL0(); BAR();
  if (wid >= 4) {
    WAITV(2);
    if (lane == 0) __hip_atomic_fetch_add((LAS unsigned*)(cst + SP_CNT), 1u, __ATOMIC_RELAXED, __HIP_MEMORY_SCOPE_WORKGROUP);
    dmaq(0); dmaq(1);
  } else {
    u32x4 ct_[12];
#pragma unroll
    for (int i = 0; i < 12; i++) ct_[i] = *(LAS u32x4*)(cst + i * 1024 + lane * 16);
    int ln0 = lane; asm volatile("" : "+v"(ln0));
    const int hh = ln0 >> 5, l31 = ln0 & 31, blk = (ln0 >> 4) & 1, q = (ln0 & 15) >> 2, p = ln0 & 3;
    const int pA = 4 * wid, pB = pA + 2;
    auto rdrow = [&](int pr) { frag2 X;
#pragma unroll
      for (int s = 0; s < 2; s++) { const unsigned off = (16 * s + 4 * hh + q) * 64 + (16 * blk + 4 * p) * 2;
        X.r[s] = cat8(trd(pls + ploff2(pr, off)), trd(pls + ploff2(pr, off + 512)));
        X.i[s] = cat8(trd(pls + ploff2(pr + 1, off)), trd(pls + ploff2(pr + 1, off + 512))); }
      return X; };
    frag2 XA = rdrow(pA), XB = rdrow(pB);
    bf16x8 Cc[2], Sn[2], nS[2];
#pragma unroll
    for (int s = 0; s < 2; s++) { Cc[s] = __builtin_bit_cast(bf16x8, ct_[s]); Sn[s] = __builtin_bit_cast(bf16x8, ct_[2 + s]); nS[s] = __builtin_bit_cast(bf16x8, ct_[2 + s] ^ 0x80008000u); }
    f32x2 twc[8], tws[8];
#pragma unroll
    for (int r = 0; r < 8; r++) { twc[r] = (f32x2){__uint_as_float(ct_[4 + (r >> 1)][(2 * r) & 3]), __uint_as_float(ct_[4 + (r >> 1)][(2 * r + 1) & 3])};
                                  tws[r] = (f32x2){__uint_as_float(ct_[8 + (r >> 1)][(2 * r) & 3]), __uint_as_float(ct_[8 + (r >> 1)][(2 * r + 1) & 3])}; }
    const f32x16 z = {0.f};
    auto stA = [&](const frag2& X, bool inv) { cplx16 c; const bf16x8* P = inv ? nS : Sn; const bf16x8* N = inv ? Sn : nS;
      c.re = MFMA32(X.r[0], Cc[0], z); c.im = MFMA32(X.i[0], Cc[0], z); c.re = MFMA32(X.r[1], Cc[1], c.re); c.im = MFMA32(X.i[1], Cc[1], c.im);
      c.re = MFMA32(X.i[0], P[0], c.re); c.im = MFMA32(X.r[0], N[0], c.im); c.re = MFMA32(X.i[1], P[1], c.re); c.im = MFMA32(X.r[1], N[1], c.im); return c; };
    auto stB = [&](const frag2& X) { cplx16 c;
      c.re = MFMA32(Cc[0], X.r[0], z); c.im = MFMA32(Cc[0], X.i[0], z); c.re = MFMA32(Cc[1], X.r[1], c.re); c.im = MFMA32(Cc[1], X.i[1], c.im);
      c.re = MFMA32(Sn[0], X.i[0], c.re); c.im = MFMA32(nS[0], X.r[0], c.im); c.re = MFMA32(Sn[1], X.i[1], c.re); c.im = MFMA32(nS[1], X.r[1], c.im); return c; };
    auto cmul_pack = [&](const cplx16& c, auto&& coef) { frag2 f; u32x4 wr_[2], wi_[2];
#pragma unroll
      for (int g = 0; g < 4; g++) { f32x2 cr0, ci0, cr1, ci1; coef(2 * g, cr0, ci0); coef(2 * g + 1, cr1, ci1);
        const f32x2 a0 = {c.re[4 * g], c.re[4 * g + 1]}, b0 = {c.im[4 * g], c.im[4 * g + 1]}, a1 = {c.re[4 * g + 2], c.re[4 * g + 3]}, b1 = {c.im[4 * g + 2], c.im[4 * g + 3]};
        f32x2 t0 = a0 * cr0, t1 = a0 * ci0, t2 = a1 * cr1, t3 = a1 * ci1;
        asm volatile("" : "+v"(t0), "+v"(t1), "+v"(t2), "+v"(t3));
        t0 = t0 - b0 * ci0; t1 = t1 + b0 * cr0; t2 = t2 - b1 * ci1; t3 = t3 + b1 * cr1;
        asm volatile("" : "+v"(t0), "+v"(t1), "+v"(t2), "+v"(t3));
        wr_[g >> 1][(2 * g) & 3] = pk2v(t0); wi_[g >> 1][(2 * g) & 3] = pk2v(t1); wr_[g >> 1][(2 * g + 1) & 3] = pk2v(t2); wi_[g >> 1][(2 * g + 1) & 3] = pk2v(t3); }
      f.r[0] = __builtin_bit_cast(bf16x8, wr_[0]); f.r[1] = __builtin_bit_cast(bf16x8, wr_[1]); f.i[0] = __builtin_bit_cast(bf16x8, wi_[0]); f.i[1] = __builtin_bit_cast(bf16x8, wi_[1]);
      return f; };
    auto twf = [&](int r, f32x2& cr, f32x2& ci) { cr = twc[r]; ci = -tws[r]; };
    auto twi = [&](int r, f32x2& cr, f32x2& ci) { cr = twc[r]; ci = tws[r]; };
    struct filt { s16x4 dr[2], di[2]; u16 mr[8], mi[8]; };
    auto fload = [&](LAS char* fs) { filt f;
#pragma unroll
      for (int G = 0; G < 2; G++) { const unsigned off = (8 * G + 4 * hh + q) * 64 + (16 * blk + 4 * p) * 2; f.dr[G] = trd(fs + off); f.di[G] = trd(fs + 2048 + off); }
#pragma unroll
      for (int r = 8; r < 16; r++) { const unsigned o = (1023 - 32 * crow(r, hh) - l31) * 2; f.mr[r - 8] = *(LAS u16*)(fs + 1024 + o); f.mi[r - 8] = *(LAS u16*)(fs + 3072 + o); }
      return f; };
    auto fval = [&](const filt& f, const unsigned* fxv, int r, float& fr, float& fi) {
      if (r < 8) { fr = bfe(f.dr[r >> 2], r & 3); fi = bfe(f.di[r >> 2], r & 3); } else { fr = bf2f(f.mr[r - 8]); fi = -bf2f(f.mi[r - 8]); }
      if (r == 0) { const float ar = 0.5f * (fr + __uint_as_float(fxv[0] << 16)), ai = 0.5f * (fi - __uint_as_float(fxv[0] & 0xffff0000u)); fr = lane == 0 ? ar : fr; fi = lane == 0 ? ai : fi; }
      if (r == 8) { const float ar = 0.5f * (fr + __uint_as_float(fxv[1] << 16)), ai = 0.5f * (fi + __uint_as_float(fxv[1] & 0xffff0000u)); fr = lane == 0 ? ar : fr; fi = lane == 0 ? ai : fi; } };
    auto wrrow = [&](int pr, const cplx16& c) {
#pragma unroll
      for (int G = 0; G < 4; G++) { const unsigned off = (32 * l31 + 8 * G + 4 * hh) * 2;
        *(LAS u32x2*)(pls + ploff3(pr, off)) = (u32x2){pk2(c.re[4 * G], c.re[4 * G + 1]), pk2(c.re[4 * G + 2], c.re[4 * G + 3])};
        *(LAS u32x2*)(pls + ploff3(pr + 1, off)) = (u32x2){pk2(c.im[4 * G], c.im[4 * G + 1]), pk2(c.im[4 * G + 2], c.im[4 * G + 3])}; } };
    LAS char* fsA = (wid < 2 ? fsa : fsb) + ((2 * wid) & 3) * 4096; LAS char* fsB = fsA + 4096;
    cplx16 A = stA(XA, false);
    SB();
    cplx16 B = stA(XB, false); XA = cmul_pack(A, twf); INTERLEAVE(6);
    A = stB(XA); XB = cmul_pack(B, twf);
    while (__builtin_amdgcn_readfirstlane(*(volatile LAS unsigned*)(cst + SP_CNT)) < 4u) __builtin_amdgcn_s_sleep(1);
    asm volatile("" ::: "memory");
    unsigned fxp[2][2];
#pragma unroll
    for (int k = 0; k < 2; k++)
#pragma unroll
      for (int i = 0; i < 2; i++) fxp[k][i] = (*(LAS unsigned*)(cst + SP_FX + (2 * (2 * wid + k) + i) * 4) & 0xffffu) | (*(LAS unsigned*)(cst + SP_FX + 256 + (2 * (2 * wid + k) + i) * 4) << 16);
    const filt fA = fload(fsA); INTERLEAVE(6);
    B = stB(XB);
    if (sp && wid == 0) {
      LAS char* sc = pls;
      const filt fC = fload(fsx);
      const unsigned fyp[2] = {(unsigned)*(LAS u16*)(fsx) | ((unsigned)*(LAS u16*)(fsx + 2048) << 16), (unsigned)*(LAS u16*)(fsx + 1024 + 1022) | ((unsigned)*(LAS u16*)(fsx + 3072 + 1022) << 16)};
      f32x16 zr;
      WAITL0(); asm volatile("" ::: "memory");
#pragma unroll
      for (int r = 0; r < 16; r++) *(LAS float*)(sc + (32 * crow(r, hh) + l31) * 4) = A.re[r];
      WAITL0(); asm volatile("" ::: "memory");
#pragma unroll
      for (int r = 0; r < 16; r++) zr[r] = *(LAS float*)(sc + ((1024 - (32 * crow(r, hh) + l31)) & 1023) * 4);
      WAITL0(); asm volatile("" : "+v"(zr) :: "memory");
#pragma unroll
      for (int r = 0; r < 16; r++) *(LAS float*)(sc + (32 * crow(r, hh) + l31) * 4) = A.im[r];
      WAITL0(); asm volatile("" ::: "memory");
#pragma unroll
      for (int r = 0; r < 16; r++) { float ar, ai, br, bi; fval(fA, fxp[0], r, ar, ai); fval(fC, fyp, r, br, bi);
        const float zi = -*(LAS float*)(sc + ((1024 - (32 * crow(r, hh) + l31)) & 1023) * 4);
        const float alr = 0.5f * (ar + br), ali = 0.5f * (ai + bi), ber = 0.5f * (ar - br), bei = 0.5f * (ai - bi);
        const float a = A.re[r], b = A.im[r];
        A.re[r] = a * alr - b * ali + zr[r] * ber - zi * bei; A.im[r] = a * ali + b * alr + zr[r] * bei + zi * ber;
        if (r & 1) { asm volatile("" : "+v"(A.re[r]), "+v"(A.im[r]), "+v"(A.re[r - 1]), "+v"(A.im[r - 1]) :: "memory"); SB(); } }
      WAITL0(); asm volatile("" ::: "memory");
      XA = packc(A);
      SB();
    } else {
      XA = cmul_pack(A, [&](int r, f32x2& cr, f32x2& ci) { float a0, b0, a1, b1; fval(fA, fxp[0], 2 * r, a0, b0); fval(fA, fxp[0], 2 * r + 1, a1, b1); cr = (f32x2){a0, a1}; ci = (f32x2){b0, b1}; });
      INTERLEAVE(11);
    }
    const filt fB = fload(fsB);
    A = stA(XA, true);
    XB = cmul_pack(B, [&](int r, f32x2& cr, f32x2& ci) { float a0, b0, a1, b1; fval(fB, fxp[1], 2 * r, a0, b0); fval(fB, fxp[1], 2 * r + 1, a1, b1); cr = (f32x2){a0, a1}; ci = (f32x2){b0, b1}; });
    INTERLEAVE(11);
    B = stA(XB, true); XA = cmul_pack(A, twi); INTERLEAVE(6);
    A = stA(XA, true); XB = cmul_pack(B, twi); INTERLEAVE(6);
    B = stA(XB, true); wrrow(pA, A); INTERLEAVE(3);
    wrrow(pB, B);
  }
  WAITL0(); BAR();
  if (wid < 4) { int ln = lane; asm volatile("" : "+v"(ln)); const int g = ln >> 4, q = (ln & 15) >> 2, p = ln & 3, n = ln & 15;
    const int row = sp ? ((n & 7) * 256 + (n >> 3)) : ((n >> 1) * 256 + 2 * dl + (n & 1));
    rsrc_t ro = mkrs(T2, 8 * 256 * 1024 * 2);
    s16x4 a0[8], a1[8]; f32x4 e0[8], e1[8];
#pragma unroll
    for (int t = 0; t < 8; t++) { const int d0 = wid * 256 + t * 32; a0[t] = trd(pls + ploff3(4 * g + q, (d0 + 8 * p) * 2)); a1[t] = trd(pls + ploff3(4 * g + q, (d0 + 8 * p + 4) * 2)); }
#pragma unroll
    for (int t = 0; t < 8; t++) {
      e0[t] = __builtin_amdgcn_mfma_f32_16x16x16bf16_1k(a0[t], Mi, (f32x4){0.f, 0.f, 0.f, 0.f}, 0, 0, 0);
      e1[t] = __builtin_amdgcn_mfma_f32_16x16x16bf16_1k(a1[t], Mi, (f32x4){0.f, 0.f, 0.f, 0.f}, 0, 0, 0); }
    SB();
#pragma unroll
    for (int t = 0; t < 8; t++) { const int d0 = wid * 256 + t * 32;
      __builtin_amdgcn_raw_buffer_store_b128((u32x4){pk2(e0[t][0], e0[t][1]), pk2(e0[t][2], e0[t][3]), pk2(e1[t][0], e1[t][1]), pk2(e1[t][2], e1[t][3])}, ro, (row * 1024 + d0 + 8 * g) * 2, 0, 16); } }
}

constexpr int G2_B = 16384, G2_W0 = 4 * G2_B, G2_PL = 8 * G2_B, G2_LDS = G2_PL + 32768;
#define K0STEPS 20
constexpr int NP = K0STEPS, NM = 56 - K0STEPS, NF = NM + 4;
extern "C" __global__ __launch_bounds__(512, 2) void k_gemm2(const u16* __restrict__ h, const u16* __restrict__ w2, const u16* __restrict__ b2, const u16* __restrict__ T,
                                                  const u16* __restrict__ wr, const u16* __restrict__ wi, u16* __restrict__ T2,
                                                  const float* __restrict__ g, u16* __restrict__ out, u16* __restrict__ part, unsigned* __restrict__ flags) {
  extern __shared__ __attribute__((aligned(16))) char smem[];
  LAS char* lds = (LAS char*)smem;
  const int tid = threadIdx.x, lane = tid & 63, wid = __builtin_amdgcn_readfirstlane(tid >> 6);
  const int b = blockIdx.x, xcd = b & 7, idx = b >> 3;
  const int kh = idx & 1, mb = (xcd & 3) * 4 + ((idx >> 1) & 3), nb = (xcd >> 2) * 4 + (idx >> 3);
  const int pair = mb * 8 + nb;
  const int beta = mb >> 1, alpha0 = (mb & 1) * 128, d0 = nb * 128;
  const bool isP = (kh == 0);
  rsrc_t rh = mkrs((const char*)h + (size_t)mb * 4 * 224 * 1024, 4 * 224 * 1024);
  rsrc_t rw = mkrs(w2 + d0, (F_ * D_ - d0) * 2);
  const unsigned vh = lane * 16;
  unsigned vw[4];
#pragma unroll
  for (int i = 0; i < 4; i++) vw[i] = (lane >> 4) * 2048 + (((lane & 15) ^ ((lane >> 4) << 2)) * 16) + i * (8192 - 1024);
  const int lw = wid - 4;
  auto stage = [&](int kk, int buf) {
    dma4(rh, lds + buf * G2_B + lw * 4096, vh, vh, vh, vh, (lw * 224 + 4 * kk) * 1024);
    dma4(rw, lds + G2_W0 + buf * G2_B + lw * 4096, vw[0], vw[1], vw[2], vw[3], (kk * 64 + lw * 16) * 2048); };
  if (isP) spectral_unit(lds + G2_PL, lds + 3 * G2_B, lds + G2_W0 + 3 * G2_B, lds + G2_W0 + 2 * G2_B, lds + 2 * G2_B, pair, wid, lane, T, wr, wi, T2, [&](int st) {
    if (st >= 0) stage(st, st);
    else { const int gwv = pair * 4 + lw;
      constexpr int LF = (56 - NP - PFS) * 64 * 16 / 512, LP = NP * 64 * 16 / 512;
      static_assert(LF <= 64 && LP <= 64, "");
      touch255((const char*)w2 + (size_t)((NP + PFS) * 64 * 16 + gwv * LF + (lane < LF ? lane : 0)) * 128);
      touch255((const char*)w2 + (size_t)(gwv * LP + (lane < LP ? lane : 0)) * 128);
    } });
  if (wid >= 4) {
    if (isP) {
      WAITV(8); BAR();
      stage(2, 2);
      WAITV(8); BAR();
      stage(3, 3); stage(4, 0);
      for (int kt = 1; kt < NP - 4; kt++) { WAITV(16); BAR(); stage(kt + 4, kt & 3); }
      WAITV(16); BAR();
      WAITV(8); BAR();
      WAITV(0); BAR();
      BAR();
      return;
    }
    rsrc_t re = mkrs((const char*)&ETAB2 + (mb & 1) * 4 * 16 * 1024, 4 * 16 * 1024);
    rsrc_t rt = mkrs(T2 + beta * 256 * 1024 + d0, (256 * 1024 - d0) * 2);
    rsrc_t rp = mkrs((const char*)part + (size_t)pair * 32768, 32768);
    stage(NP + 0, 0); stage(NP + 1, 1);
    WAITV(8); BAR();
    stage(NP + 2, 2); stage(NP + 3, 3);
    WAITV(16); BAR(); stage(NP + 4, 0);
    for (int kt = 1; kt < NM - 4; kt++) { WAITV(16); BAR(); stage(NP + kt + 4, kt & 3); }
#pragma unroll
    for (int k2 = 0; k2 < 4; k2++) {
      WAITV(16); BAR();
      constexpr int dummy = 0; (void)dummy;
      const int buf = (NM - 4 + k2) & 3;
      dma4(re, lds + buf * G2_B + lw * 4096, vh, vh, vh, vh, (lw * 16 + 4 * k2) * 1024);
      dma4(rt, lds + G2_W0 + buf * G2_B + lw * 4096, vw[0], vw[1], vw[2], vw[3], (k2 * 64 + lw * 16) * 2048);
    }
    WAITV(16); BAR();
    dma4(rp, lds + G2_PL + lw * 8192, vh, vh, vh, vh, lw * 8192);
    dma4(rp, lds + G2_PL + lw * 8192 + 4096, vh, vh, vh, vh, lw * 8192 + 4096);
    WAITV(16); BAR();
    WAITV(8); BAR();
    WAITV(0); BAR();
    return;
  }
  const int wm = wid >> 1, wn = wid & 1;
  const int hh = lane >> 5, l31 = lane & 31;
  LAS char* ha = lds + (2 * wm) * 4096 + lane * 16;
  unsigned wa[2];
  { const int blk = (lane >> 4) & 1, q = (lane & 15) >> 2, p = lane & 3;
    const unsigned a0 = G2_W0 + (8 * hh + q) * 256 + ((((wn << 3) | (blk << 1) | (p >> 1)) ^ (q << 2)) << 4) + ((p & 1) << 3);
    wa[0] = a0; wa[1] = a0 ^ 64; }
  f32x16 acc[2][2];
#pragma unroll
  for (int t = 0; t < 2; t++)
#pragma unroll
    for (int i = 0; i < 2; i++) acc[t][i] = (f32x16){0.f};
  bf16x8 hf[2][2];
  s16x4 wl[2][2], wh[2][2];
#define G2RD(ns, buf, ks, n) do { if ((n) < 2) hf[ns][(n) & 1] = ld128(ha + (buf) * G2_B + ((n) & 1) * 4096 + (ks) * 1024); \
    else if (((n) & 1) == 0) wl[ns][(((n) - 2) >> 1) & 1] = trd(lds + wa[(((n) - 2) >> 1) & 1] + (buf) * G2_B + (ks) * 4096); \
    else wh[ns][(((n) - 2) >> 1) & 1] = trd(lds + wa[(((n) - 2) >> 1) & 1] + (buf) * G2_B + (ks) * 4096 + 1024); } while (0)
#define G2SUB(A, cs, ns, buf, ks, DO_RD) do { \
    if (DO_RD) { G2RD(ns, buf, ks, 0); G2RD(ns, buf, ks, 2); } A[0][0] = MFMA32(cat8(wl[cs][0], wh[cs][0]), hf[cs][0], A[0][0]); SB(); \
    if (DO_RD) { G2RD(ns, buf, ks, 3); G2RD(ns, buf, ks, 1); } A[0][1] = MFMA32(cat8(wl[cs][0], wh[cs][0]), hf[cs][1], A[0][1]); SB(); \
    if (DO_RD) { G2RD(ns, buf, ks, 4); G2RD(ns, buf, ks, 5); } A[1][0] = MFMA32(cat8(wl[cs][1], wh[cs][1]), hf[cs][0], A[1][0]); SB(); \
    A[1][1] = MFMA32(cat8(wl[cs][1], wh[cs][1]), hf[cs][1], A[1][1]); SB(); } while (0)
#define G2STEPW(A, buf, AW) do { G2SUB(A, 0, 1, buf, 1, true); G2SUB(A, 1, 0, buf, 2, true); G2SUB(A, 0, 1, buf, 3, true); WAITL0(); AW; BAR(); SB(); \
    G2SUB(A, 1, 0, ((buf) + 1) & 3, 0, true); } while (0)
#define G2STEP(A, buf) G2STEPW(A, buf, (void)0)
#define G2LAST(A, buf) do { G2SUB(A, 0, 1, buf, 1, true); G2SUB(A, 1, 0, buf, 2, true); G2SUB(A, 0, 1, buf, 3, true); G2SUB(A, 1, 0, 0, 0, false); } while (0)
  float gg[2];
  if (!isP) {
#pragma unroll
    for (int i = 0; i < 2; i++) gg[i] = g[256 * beta + alpha0 + 64 * wm + 32 * i + l31];
#pragma unroll
    for (int t = 0; t < 2; t++)
#pragma unroll
      for (int G = 0; G < 4; G++) { const u32x2 bv = *(const u32x2*)(b2 + d0 + 64 * wn + 32 * t + 8 * G + 4 * hh);
#pragma unroll
        for (int i = 0; i < 2; i++) { acc[t][i][4 * G] = __uint_as_float(bv[0] << 16); acc[t][i][4 * G + 1] = __uint_as_float(bv[0] & 0xffff0000u);
                                      acc[t][i][4 * G + 2] = __uint_as_float(bv[1] << 16); acc[t][i][4 * G + 3] = __uint_as_float(bv[1] & 0xffff0000u); } }
  }
  const int nloop = isP ? NP - 1 : NM;
  rsrc_t rf = mkrs(flags, (FLAG_PAIR + 32 * 128) * 4);
  auto pre = [&](int st) {
    if (isP) { if (st == 2) WAITV(0); }
    else if (wid == 0) {
      const bool second = st >= NM - 2;
      const int word = second ? FLAG_PAIR + 32 * pair : FLAG_BDONE, first = second ? NM - 2 : NM - 6;
      if (st == first) asm volatile("s_mov_b32 m0, %0\n\ts_nop 0\n\tbuffer_load_dword %1, %2, %3 offen sc1 lds" :: "s"((unsigned)(size_t)(lds + G2_PL)), "v"(0u), "s"(rf), "s"(word * 4) : "memory");
      else if (st == first + 1) {
        WAITV(0);
        const unsigned want = second ? 1u : 128u;
        unsigned spins = 0, cur = __builtin_amdgcn_readfirstlane(*(volatile LAS unsigned*)(lds + G2_PL));
        while (cur < want) { if (++spins > (1u << 22)) break; __builtin_amdgcn_s_sleep(1);
          cur = __builtin_amdgcn_readfirstlane(__hip_atomic_load((gu32*)(flags + word), __ATOMIC_RELAXED, __HIP_MEMORY_SCOPE_AGENT)); }
        __builtin_amdgcn_fence(__ATOMIC_ACQUIRE, "agent");
      } else if (st == first + 2) WAITV(0);
    } };
  BAR();
#pragma unroll
  for (int n = 0; n < 6; n++) G2RD(0, 0, 0, n);
#define G2ONE(buf) do { G2STEPW(acc, buf, pre(st)); \
    if (isP && st == 2 && tid == 0) __hip_atomic_fetch_add((gu32*)(flags + FLAG_BDONE), 1u, __ATOMIC_RELAXED, __HIP_MEMORY_SCOPE_AGENT); \
    st++; } while (0)
#define G2EXIT(buf) (((((NP - 1) & 3) == (((buf) + 1) & 3)) || ((NM & 3) == (((buf) + 1) & 3))) && st == nloop)
  for (int st = 0;;) {
    G2ONE(0); if (G2EXIT(0)) break;
    G2ONE(1); if (G2EXIT(1)) break;
    G2ONE(2); if (G2EXIT(2)) break;
    G2ONE(3); if (G2EXIT(3)) break;
  }
  if (isP) {
    G2LAST(acc, (NP - 1) & 3);
    rsrc_t rp = mkrs((const char*)part + (size_t)pair * 32768, 32768);
    const unsigned po = wid * 8192 + lane * 16;
#pragma unroll
    for (int t = 0; t < 2; t++)
#pragma unroll
      for (int i = 0; i < 2; i++)
#pragma unroll
        for (int s = 0; s < 2; s++) __builtin_amdgcn_raw_buffer_store_b128(__builtin_bit_cast(u32x4, pack8(acc[t][i], s)), rp, po + ((t * 2 + i) * 2 + s) * 1024, 0, 16);
    WAITV(0); BAR();
    if (tid == 0) __hip_atomic_store((gu32*)(flags + FLAG_PAIR + 32 * pair), 1u, __ATOMIC_RELAXED, __HIP_MEMORY_SCOPE_AGENT);
    return;
  }
  f32x16 aca[2][2];
#pragma unroll
  for (int t = 0; t < 2; t++)
#pragma unroll
    for (int i = 0; i < 2; i++) aca[t][i] = (f32x16){0.f};
  G2STEPW(aca, NM & 3, pre(NM));
  G2STEP(aca, (NM + 1) & 3);
  G2STEP(aca, (NM + 2) & 3);
  G2LAST(aca, (NM + 3) & 3);
  BAR();
  LAS char* pb = lds + G2_PL + wid * 8192 + lane * 16;
  rsrc_t ro_ = mkrs(out, S_ * D_ * 2);
  u32x4 pp[2][2][2];
#pragma unroll
  for (int t = 0; t < 2; t++)
#pragma unroll
    for (int i = 0; i < 2; i++)
#pragma unroll
      for (int s = 0; s < 2; s++) pp[t][i][s] = *(LAS u32x4*)(pb + ((t * 2 + i) * 2 + s) * 1024);
  SB();
#pragma unroll
  for (int i = 0; i < 2; i++) {
    const int al = alpha0 + 64 * wm + 32 * i + l31;
    const float gv = gg[i], og = rbf(1.f - gv);
    const unsigned oo = ((8 * al + beta) * D_ + d0 + 64 * wn + 8 * hh) * 2;
#pragma unroll
    for (int t = 0; t < 2; t++) {
      unsigned o[8];
#pragma unroll
      for (int s = 0; s < 2; s++)
#pragma unroll
        for (int e = 0; e < 4; e++) { const int r = 8 * s + 2 * e; const unsigned pw = pp[t][i][s][e];
          const f32x2 pv2 = {__uint_as_float(pw << 16), __uint_as_float(pw & 0xffff0000u)};
          const f32x2 a = {aca[t][i][r], aca[t][i][r + 1]}, c = {acc[t][i][r], acc[t][i][r + 1]};
          o[4 * s + e] = pk2v(a * gv + (c + pv2) * og); }
#pragma unroll
      for (int G = 0; G < 4; G += 2) {
        const auto s0 = __builtin_amdgcn_permlane32_swap(o[2 * G], o[2 * G + 2], false, false);
        const auto s1 = __builtin_amdgcn_permlane32_swap(o[2 * G + 1], o[2 * G + 3], false, false);
        __builtin_amdgcn_raw_buffer_store_b128((u32x4){s0[0], s1[0], s0[1], s1[1]}, ro_, oo + (32 * t + 8 * G) * 2, 0, OUT_AUX);
      }
    }
  }
}

extern "C" void kernel_launch(void* const* d_in, const int* in_sizes, int n_in, void* d_out, int out_size, void* d_ws, size_t ws_size, hipStream_t stream) {
  const u16* x = (const u16*)d_in[0]; const u16* wr = (const u16*)d_in[1]; const u16* wi = (const u16*)d_in[2];
  const u16* w1 = (const u16*)d_in[4]; const u16* b1 = (const u16*)d_in[5]; const u16* w2 = (const u16*)d_in[6]; const u16* b2 = (const u16*)d_in[7];
  const u16* gw = (const u16*)d_in[8];
  char* ws = (char*)d_ws;
  float* g = (float*)ws;
  unsigned* flags = (unsigned*)(ws + (64 << 10));
  u16* T = (u16*)(ws + (1 << 20));
  u16* T2 = (u16*)(ws + (5 << 20));
  u16* h = (u16*)(ws + (9 << 20));
  u16* part = (u16*)(ws + (24 << 20));
  (void)hipFuncSetAttribute((const void*)k_gemm1, hipFuncAttributeMaxDynamicSharedMemorySize, G1_LDS);
  (void)hipFuncSetAttribute((const void*)k_gemm2, hipFuncAttributeMaxDynamicSharedMemorySize, G2_LDS);
  k_gemm1<<<256, 512, G1_LDS, stream>>>(x, w1, b1, gw, h, T, g, flags, w2, wr, wi);
  k_gemm2<<<256, 512, G2_LDS, stream>>>(h, w2, b2, T, wr, wi, T2, g, (u16*)d_out, part, flags);
}
```
